```python
import torch

def _softmax(x, axis=-1):
    return torch.softmax(x, dim=axis)
def _gelu(x):
    return torch.nn.functional.gelu(x, approximate="tanh")
def _silu(x):
    return torch.nn.functional.silu(x)
def _sigmoid(x):
    return torch.sigmoid(x)
def _layernorm(x, axis=-1, eps=1e-5):
    m = x.mean(dim=axis, keepdim=True)
    v = x.var(dim=axis, keepdim=True, unbiased=False)
    return (x - m) / torch.sqrt(v + eps)
def _scatter_add(tgt, idx, src, axis=0):
    return tgt.clone().index_add_(axis, idx, src)
def _segment_sum(src, seg, n):
    out = torch.zeros((n,) + src.shape[1:], dtype=src.dtype)
    return out.index_add_(0, seg, src)
def _pad(x, pw):
    flat = tuple(v for pair in reversed(pw) for v in pair)
    return torch.nn.functional.pad(x, flat)

kernel_name = 'standalone__normalfloat_lut_nf4__prompt'

def setup_inputs():

    _w = (lambda a: (lambda t: t.to(torch.bfloat16) if t.is_floating_point() else t)(torch.as_tensor(a)))
    B, S, D, F = 1, 2048, 576, 4608
    G = -1 if -1 > 0 else D
    D = (D // G) * G
    ng = D // G
    qw = torch.randint(0, 16, (D, F)).to(torch.int64)
    return {
        "x": _w((lambda *s: torch.randn(*s, dtype=torch.bfloat16) * 0.05)(B, S, D)),
        "qw": _w(qw),
        "absmax": _w(torch.abs((lambda *s: torch.randn(*s, dtype=torch.bfloat16) * 0.05)(ng, F)).to(float) + 0.01),
    }

def reference(x, qw, absmax):
    NF4 = torch.as_tensor([
        -1.0, -0.6961928009986877, -0.5250730514526367, -0.39491748809814453,
        -0.28444138169288635, -0.18477343022823334, -0.09105003625154495, 0.0,
        0.07958029955625534, 0.16093020141124725, 0.24611230194568634,
        0.33791524171829224, 0.44070982933044434, 0.5626170039176941,
        0.7229568362236023, 1.0,
    ])
    D, F = qw.shape[0], qw.shape[1]
    ng = absmax.shape[0]
    G = D // ng
    w = (lambda x, dt: x.to(dt))(NF4[qw], x.dtype)
    s = (lambda x, n, axis: torch.repeat_interleave(x, n, dim=axis))(absmax, G, axis=0)
    w = w * s
    return torch.einsum("bsd,df->bsf", x, w)
```

```cpp
#include <hip/hip_runtime.h>
#include <hip/hip_bf16.h>
typedef __attribute__((ext_vector_type(8))) short bf16x8;
typedef __attribute__((ext_vector_type(4))) float f32x4;
typedef __attribute__((ext_vector_type(4))) int i32x4;
constexpr int M=2048,K=576,N=4608;
__constant__ unsigned short NF4B[16]={0xBF80,0xBF32,0xBF06,0xBECA,0xBE92,0xBE3D,0xBDBA,0x0000,0x3DA3,0x3E25,0x3E7C,0x3EAD,0x3EE2,0x3F10,0x3F39,0x3F80};
__device__ __forceinline__ unsigned short f2bf(float f){ __hip_bfloat16 h=__float2bfloat16(f); return *(unsigned short*)&h; }
__global__ __launch_bounds__(256) void k0(const unsigned short* __restrict__ x,const int* __restrict__ qw,const unsigned short* __restrict__ am,unsigned short* __restrict__ out){
  __shared__ unsigned short lut[16];
  int tid=threadIdx.x; if(tid<16) lut[tid]=NF4B[tid];
  __syncthreads();
  int lane=tid&63,w=tid>>6,c=lane&15,g=lane>>4;
  int m0=blockIdx.y*64, n0=blockIdx.x*256+w*64;
  f32x4 acc[4][4]={};
  for(int k0=0;k0<K;k0+=32){
    i32x4 q[8];
    #pragma unroll
    for(int j=0;j<8;j++) q[j]=*(const i32x4*)(qw+(size_t)(k0+8*g+j)*N+n0+4*c);
    bf16x8 a[4];
    #pragma unroll
    for(int i=0;i<4;i++) a[i]=*(const bf16x8*)(x+(size_t)(m0+i*16+c)*K+k0+8*g);
    #pragma unroll
    for(int t=0;t<4;t++){
      bf16x8 b;
      #pragma unroll
      for(int j=0;j<8;j++) b[j]=lut[q[j][t]&15];
      #pragma unroll
      for(int i=0;i<4;i++) acc[i][t]=__builtin_amdgcn_mfma_f32_16x16x32_bf16(b,a[i],acc[i][t],0,0,0);
    }
  }
  #pragma unroll
  for(int i=0;i<4;i++){
    int m=m0+i*16+c;
    #pragma unroll
    for(int r=0;r<4;r++){
      #pragma unroll
      for(int t=0;t<4;t++){
        int n=n0+16*g+4*r+t;
        float s=__uint_as_float(((unsigned)am[n])<<16);
        out[(size_t)m*N+n]=f2bf(acc[i][t][r]*s);
      }
    }
  }
}
extern "C" void kernel_launch(void* const* d_in,const int* in_sizes,int n_in,void* d_out,int out_size,void* d_ws,size_t ws_size,hipStream_t stream){
  k0<<<dim3(N/256,M/64),256,0,stream>>>((const unsigned short*)d_in[0],(const int*)d_in[1],(const unsigned short*)d_in[2],(unsigned short*)d_out);
}
```

```cpp
#include <hip/hip_runtime.h>

__global__ __launch_bounds__(256, 1) __attribute__((aligned(4096))) void nf4_gemm(const void* x, const void* q, const void* am, void* out, void* ws) {
  __shared__ __attribute__((aligned(16))) char lds[163840];
  asm volatile(
    "s_load_dwordx8 s[84:91], s[0:1], 0x0\n\t"
    "s_load_dwordx2 s[92:93], s[0:1], 0x20\n\t"
    "v_readfirstlane_b32 s72, v0\n\t"
    "s_and_b32 s29, s2, 1\n\t"
    "s_lshr_b32 s73, s2, 3\n\t"
    "s_add_u32 s74, s73, s29\n\t"
    "s_cmp_ge_u32 s74, 32\n\t"
    "s_cbranch_scc1 L_exit_%=\n\t"
    "s_lshl_b32 s29, s29, 5\n\t"
    "s_add_u32 s29, s29, s73\n\t"
    "s_mul_i32 s69, s29, 7282\n\t"
    "s_lshr_b32 s69, s69, 16\n\t"
    "s_bfe_u32 s74, s2, 0x20001\n\t"
    "s_sub_u32 s74, s74, s69\n\t"
    "s_mul_i32 s74, s74, 9\n\t"
    "s_add_u32 s68, s29, s74\n\t"
    "s_lshr_b32 s72, s72, 6\n\t"
    "s_mul_i32 s29, s68, 3\n\t"
    "s_bfe_u32 s74, s2, 0x20001\n\t"
    "s_add_u32 s29, s29, s74\n\t"
    "s_mul_i32 s74, s29, 7282\n\t"
    "s_lshr_b32 s74, s74, 16\n\t"
    "s_mul_i32 s74, s74, 9\n\t"
    "s_sub_u32 s27, s29, s74\n\t"
    "s_and_b32 s25, s2, 1\n\t"
    "s_lshl_b32 s25, s25, 2\n\t"
    "s_or_b32 s25, s25, s69\n\t"
    "s_lshl2_add_u32 s25, s25, 0x40000\n\t"
    "s_bfe_u32 s24, 0x66330210, s25\n\t"
    "s_add_u32 s24, s27, s24\n\t"
    "s_sub_u32 s29, s24, 9\n\t"
    "s_min_u32 s24, s24, s29\n\t"
    "s_lshl2_add_u32 s71, s68, s72\n\t"
    "s_lshl_b32 s71, s71, 7\n\t"
    "s_mul_i32 s43, s24, 1179648\n\t"
    "v_bfe_u32 v236, v0, 4, 2\n\t"
    "v_and_or_b32 v233, v0, 4, v236\n\t"
    "v_mul_u32_u24 v233, 147456, v233\n\t"
    "v_and_b32 v234, 10, v0\n\t"
    "v_lshl_add_u32 v234, v234, 3, s71\n\t"
    "v_and_b32 v235, 1, v0\n\t"
    "v_lshl_or_b32 v233, v235, 5, v233\n\t"
    "v_add_u32 v1, v233, v234\n\t"
    "s_mov_b32 s14, 1179648\n\t"
    "s_mov_b32 s15, 0x20000\n\t"
    "v_add_u32 v16, 129024, v1\n\t"
    "v_add_u32 v15, 110592, v1\n\t"
    "v_add_u32 v14, 92160, v1\n\t"
    "v_add_u32 v13, 73728, v1\n\t"
    "v_add_u32 v12, 55296, v1\n\t"
    "v_add_u32 v11, 36864, v1\n\t"
    "v_add_u32 v10, 18432, v1\n\t"
    "v_and_b32 v239, 63, v0\n\t"
    "v_sub_u32 v233, 8, v239\n\t"
    "v_and_b32 v233, 0x40000000, v233\n\t"
    "s_add_u32 s29, s71, 1331200\n\t"
    "v_lshl_add_u32 v23, v239, 2, s29\n\t"
    "v_or_b32 v23, v23, v233\n\t"
    "s_mov_b32 s94, 13631488\n\t"
    "s_mov_b32 s95, 0x20000\n\t"
    "s_waitcnt lgkmcnt(0)\n\t"
    "s_add_u32 s12, s43, s86\n\t"
    "s_addc_u32 s13, s87, 0\n\t"
    "buffer_load_dwordx4 v[92:95], v16, s[12:15], 0 offen nt\n\t"
    "buffer_load_dwordx4 v[88:91], v15, s[12:15], 0 offen nt\n\t"
    "s_load_dword s65, s[92:93], 0x0 ; address-translation warm-up only, value unused\n\t"
    "s_load_dword s65, s[84:85], 0x0 ; address-translation warm-up only, value unused\n\t"
    "s_mov_b32 s96, 2359292\n\t"
    "s_load_dword s65, s[84:85], s96 ; address-translation warm-up only, value unused\n\t"
    "buffer_load_dwordx4 v[84:87], v14, s[12:15], 0 offen nt\n\t"
    "buffer_load_dwordx4 v[80:83], v13, s[12:15], 0 offen nt\n\t"
    "s_lshl_b32 s34, s24, 7\n\t"
    "s_min_u32 s29, s69, 1\n\t"
    "s_mul_i32 s70, s69, 18\n\t"
    "s_add_u32 s70, s70, s29\n\t"
    "s_lshl_b32 s70, s70, 4\n\t"
    "s_mul_i32 s29, s72, 32\n\t"
    "s_add_u32 s29, s29, s70\n\t"
    "v_bfe_u32 v233, v0, 3, 3\n\t"
    "v_add_u32 v233, s29, v233\n\t"
    "v_mul_u32_u24 v233, 1152, v233\n\t"
    "v_and_b32 v235, 7, v0\n\t"
    "v_bfe_u32 v234, v0, 4, 2\n\t"
    "v_xor_b32 v234, v234, v235\n\t"
    "v_lshl_or_b32 v2, v234, 4, v233\n\t"
    "buffer_load_dwordx4 v[76:79], v12, s[12:15], 0 offen nt\n\t"
    "s_mov_b32 s10, 2358272\n\t"
    "s_mov_b32 s11, 0x20000\n\t"
    "s_mul_i32 s26, s72, 4096\n\t"
    "v_xor_b32 v233, 64, v2\n\t"
    "v_add_u32 v17, 8192, v233\n\t"
    "v_add_u32 v18, 16384, v2\n\t"
    "v_add_u32 v19, 24576, v233\n\t"
    "s_add_u32 s62, 147456, s84\n\t"
    "s_addc_u32 s63, s85, 0\n\t"
    "s_getpc_b64 s[76:77]\n\t"
    "s_and_b32 s76, s76, 0xfffff000\n\t"
    "s_mov_b32 s78, 19868\n\t"
    "s_mov_b32 s79, 0x20000\n\t"
    "s_lshr_b32 s29, s2, 3\n\t"
    "s_lshl_b32 s29, s29, 10\n\t"
    "buffer_load_dword v21, v23, s[92:95], 0 offen sc1 ; hand-off flags as found at entry\n\t"
    "buffer_load_dwordx4 v[72:75], v11, s[12:15], 0 offen nt\n\t"
    "v_lshrrev_b32 v238, 3, v0\n\t"
    "v_lshlrev_b32 v238, 20, v238\n\t"
    "v_lshl_add_u32 v238, v0, 7, v238\n\t"
    "buffer_load_dword v62, v238, s[76:79], s29 offen\n\t"
    "s_bfe_u32 s35, 0x78874455, s25\n\t"
    "s_add_u32 s35, s35, s27\n\t"
    "s_sub_u32 s44, s35, 9\n\t"
    "s_min_u32 s35, s35, s44\n\t"
    "s_mul_i32 s44, s35, 1179648\n\t"
    "s_lshl_b32 s35, s35, 7\n\t"
    "v_bfe_u32 v234, v0, 4, 3\n\t"
    "v_lshl_or_b32 v233, v234, 16, v235\n\t"
    "v_or_b32 v233, 0x0c000c00, v233\n\t"
    "v_mov_b32 v48, 0xca063280\n\t"
    "buffer_load_dwordx4 v[68:71], v10, s[12:15], 0 offen nt\n\t"
    "v_mov_b32 v49, 0xba3d92\n\t"
    "v_mov_b32 v50, 0xad7c25a3\n\t"
    "v_mov_b32 v51, 0x803910e2\n\t"
    "v_mov_b32 v52, 0xbebfbfbf\n\t"
    "v_mov_b32 v53, 0xbdbebe\n\t"
    "v_mov_b32 v54, 0x3e3e3e3d\n\t"
    "v_mov_b32 v55, 0x3f3f3f3e\n\t"
    "v_perm_b32 v48, v49, v48, v233\n\t"
    "v_perm_b32 v50, v51, v50, v233\n\t"
    "v_perm_b32 v52, v53, v52, v233\n\t"
    "v_perm_b32 v54, v55, v54, v233\n\t"
    "v_lshl_or_b32 v48, v52, 8, v48\n\t"
    "v_lshl_or_b32 v50, v54, 8, v50\n\t"
    "v_bfe_i32 v233, v0, 3, 1\n\t"
    "buffer_load_dwordx4 v[64:67], v1, s[12:15], 0 offen nt\n\t"
    "v_bfe_i32 v238, v0, 7, 1\n\t"
    "v_and_b32 v233, 0xffff, v233\n\t"
    "v_lshl_or_b32 v233, v238, 16, v233\n\t"
    "v_bfi_b32 v56, v233, v50, v48\n\t"
    "v_mov_b32 v57, v56\n\t"
    "v_mov_b32 v58, v56\n\t"
    "v_mov_b32 v59, v56\n\t"
    "v_lshlrev_b32 v233, 4, v235\n\t"
    "v_lshl_or_b32 v7, v0, 8, v233\n\t"
    "ds_write_b128 v7, v[56:59]\n\t"
    "v_xor_b32 v61, 16, v7\n\t"
    "ds_write_b128 v61, v[56:59]\n\t"
    "v_xor_b32 v60, 32, v7\n\t"
    "ds_write_b128 v60, v[56:59]\n\t"
    "v_xor_b32 v61, 48, v7\n\t"
    "ds_write_b128 v61, v[56:59]\n\t"
    "v_xor_b32 v60, 64, v7\n\t"
    "ds_write_b128 v60, v[56:59]\n\t"
    "s_add_u32 s8, s34, s84\n\t"
    "s_addc_u32 s9, s85, 0\n\t"
    "s_add_u32 m0, s26, 65536\n\t"
    "s_nop 0\n\t"
    "buffer_load_dwordx4 v2, s[8:11], 0 offen offset:0 lds\n\t"
    "v_xor_b32 v61, 80, v7\n\t"
    "ds_write_b128 v61, v[56:59]\n\t"
    "v_xor_b32 v60, 96, v7\n\t"
    "ds_write_b128 v60, v[56:59]\n\t"
    "v_xor_b32 v61, 112, v7\n\t"
    "ds_write_b128 v61, v[56:59]\n\t"
    "buffer_load_dwordx4 v17, s[8:11], 0 offen offset:1024 lds\n\t"
    "v_and_b32 v237, 15, v0\n\t"
    "s_lshr_b32 s29, s71, 1\n\t"
    "v_lshl_add_u32 v5, v237, 2, s29\n\t"
    "s_mov_b64 s[20:21], s[88:89]\n\t"
    "s_mov_b32 s22, 9216\n\t"
    "s_mov_b32 s23, 0x20000\n\t"
    "s_mov_b32 s56, 0x55555555\n\t"
    "s_mov_b32 s57, 0x55555555\n\t"
    "s_mov_b32 s58, 0xaaaaaaaa\n\t"
    "s_mov_b32 s59, 0xaaaaaaaa\n\t"
    "s_mov_b32 s60, 0xf0f0f0f0\n\t"
    "s_mov_b32 s61, 0xf0f0f0f0\n\t"
    "s_mov_b32 s52, 0x0c0c0400\n\t"
    "s_mov_b32 s53, 0x0c0c0500\n\t"
    "buffer_load_dwordx4 v18, s[8:11], 0 offen offset:2048 lds\n\t"
    "s_mov_b32 s54, 0x0c0c0600\n\t"
    "s_mov_b32 s55, 0x0c0c0700\n\t"
    "v_and_b32 v8, 31, v0\n\t"
    "v_lshlrev_b32 v8, 2, v8\n\t"
    "v_lshrrev_b32 v233, 1, v237\n\t"
    "v_xor_b32 v233, v233, v236\n\t"
    "v_lshlrev_b32 v233, 4, v233\n\t"
    "v_lshl_or_b32 v3, v237, 7, v233\n\t"
    "v_add_u32 v24, 65536, v3\n\t"
    "v_xor_b32 v25, 64, v24\n\t"
    "s_mul_i32 s29, s68, 36\n\t"
    "s_add_u32 s29, s29, s72\n\t"
    "s_add_u32 s29, s29, 4\n\t"
    "s_lshl_b32 s29, s29, 10\n\t"
    "buffer_load_dwordx4 v19, s[8:11], 0 offen offset:3072 lds\n\t"
    "v_lshl_add_u32 v63, v239, 4, s29\n\t"
    "s_bfe_u32 s29, 0xf6f3f210, s25\n\t"
    "v_xor_b32 v233, s29, v239\n\t"
    "v_lshl_or_b32 v6, v233, 24, v23\n\t"
    "s_add_u32 s29, s29, 1\n\t"
    "s_lshr_b32 s29, s29, 4\n\t"
    "s_lshl_b32 s29, s29, 30\n\t"
    "v_or_b32 v250, s29, v63\n\t"
    "s_mov_b32 s33, 32\n\t"
    "s_bfe_u32 s31, 0x0, s25\n\t"
    "s_bfe_u32 s29, 0x444444, s25\n\t"
    "s_lshl4_add_u32 s31, s31, s29\n\t"
    "s_bfe_u32 s29, 0xff77ebde, s25\n\t"
    "s_lshl4_add_u32 s31, s31, s29\n\t"
    "s_add_u32 s8, s34, s62\n\t"
    "s_addc_u32 s9, s63, 0\n\t"
    "s_add_u32 m0, s26, 81920\n\t"
    "s_nop 0\n\t"
    "buffer_load_dwordx4 v2, s[8:11], 0 offen offset:0 lds\n\t"
    "s_bfe_u32 s29, 0xff87f45f, s25\n\t"
    "v_xor_b32 v233, s29, v239\n\t"
    "v_lshl_or_b32 v247, v233, 24, v23\n\t"
    "s_add_u32 s29, s29, 1\n\t"
    "s_lshr_b32 s29, s29, 4\n\t"
    "s_lshl_b32 s29, s29, 30\n\t"
    "v_or_b32 v246, s29, v63\n\t"
    "v_mov_b32 v249, v63\n\t"
    "s_bfe_u32 s43, 0x10011111, s25\n\t"
    "s_bfe_u32 s29, 0x3bb3aa99, s25\n\t"
    "s_lshl4_add_u32 s43, s43, s29\n\t"
    "s_bfe_u32 s29, 0x0, s25\n\t"
    "s_lshl4_add_u32 s43, s43, s29\n\t"
    "s_bfe_u32 s36, 0x21122001, s25\n\t"
    "s_waitcnt vmcnt(13)\n\t"
    "v_lshl_add_u64 v[36:37], v[92:93], 4, v[88:89]\n\t"
    "v_lshl_add_u64 v[38:39], v[94:95], 4, v[90:91]\n\t"
    "buffer_load_dwordx4 v17, s[8:11], 0 offen offset:1024 lds\n\t"
    "s_add_u32 s36, s36, s27\n\t"
    "s_sub_u32 s45, s36, 9\n\t"
    "s_min_u32 s36, s36, s45\n\t"
    "s_mul_i32 s45, s36, 4096\n\t"
    "s_lshl_b32 s36, s36, 7\n\t"
    "s_bfe_u32 s37, 0x1122, s25\n\t"
    "s_add_u32 s37, s37, s27\n\t"
    "s_sub_u32 s46, s37, 9\n\t"
    "s_min_u32 s37, s37, s46\n\t"
    "s_mul_i32 s46, s37, 4096\n\t"
    "s_lshl_b32 s37, s37, 7\n\t"
    "s_bfe_u32 s38, 0x12213333, s25\n\t"
    "s_add_u32 s38, s38, s27\n\t"
    "s_sub_u32 s47, s38, 9\n\t"
    "s_waitcnt vmcnt(13)\n\t"
    "v_lshl_add_u64 v[36:37], v[36:37], 4, v[84:85]\n\t"
    "v_lshl_add_u64 v[38:39], v[38:39], 4, v[86:87]\n\t"
    "buffer_load_dwordx4 v18, s[8:11], 0 offen offset:2048 lds\n\t"
    "s_min_u32 s38, s38, s47\n\t"
    "s_mul_i32 s47, s38, 4096\n\t"
    "s_lshl_b32 s38, s38, 7\n\t"
    "s_bfe_u32 s39, 0x33666666, s25\n\t"
    "s_add_u32 s39, s39, s27\n\t"
    "s_sub_u32 s48, s39, 9\n\t"
    "s_min_u32 s39, s39, s48\n\t"
    "s_mul_i32 s48, s39, 4096\n\t"
    "s_lshl_b32 s39, s39, 7\n\t"
    "s_bfe_u32 s40, 0x55555544, s25\n\t"
    "s_add_u32 s40, s40, s27\n\t"
    "s_sub_u32 s49, s40, 9\n\t"
    "s_min_u32 s40, s40, s49\n\t"
    "s_mul_i32 s49, s40, 4096\n\t"
    "s_waitcnt vmcnt(13)\n\t"
    "v_lshl_add_u64 v[36:37], v[36:37], 4, v[80:81]\n\t"
    "v_lshl_add_u64 v[38:39], v[38:39], 4, v[82:83]\n\t"
    "buffer_load_dwordx4 v19, s[8:11], 0 offen offset:3072 lds\n\t"
    "s_lshl_b32 s40, s40, 7\n\t"
    "s_bfe_u32 s41, 0x44447777, s25\n\t"
    "s_add_u32 s41, s41, s27\n\t"
    "s_sub_u32 s50, s41, 9\n\t"
    "s_min_u32 s41, s41, s50\n\t"
    "s_mul_i32 s50, s41, 4096\n\t"
    "s_lshl_b32 s41, s41, 7\n\t"
    "s_bfe_u32 s42, 0x87788888, s25\n\t"
    "s_add_u32 s42, s42, s27\n\t"
    "s_sub_u32 s51, s42, 9\n\t"
    "s_min_u32 s42, s42, s51\n\t"
    "s_mul_i32 s51, s42, 4096\n\t"
    "s_lshl_b32 s42, s42, 7\n\t"
    "v_mul_u32_u24 v233, 36864, v236\n\t"
    "s_waitcnt vmcnt(13)\n\t"
    "v_lshl_add_u64 v[36:37], v[36:37], 4, v[76:77]\n\t"
    "v_lshl_add_u64 v[38:39], v[38:39], 4, v[78:79]\n\t"
    "s_add_u32 s12, s44, s86\n\t"
    "s_addc_u32 s13, s87, 0\n\t"
    "buffer_load_dwordx4 v[124:127], v16, s[12:15], 0 offen nt\n\t"
    "v_add_u32 v4, v233, v5\n\t"
    "s_mov_b32 s18, 147456\n\t"
    "s_mov_b32 s19, 0x20000\n\t"
    "s_mul_i32 s25, s70, 9216\n\t"
    "s_lshr_b32 s28, 65, s69\n\t"
    "s_and_b32 s28, s28, 1\n\t"
    "s_add_u32 s16, s25, s90\n\t"
    "s_addc_u32 s17, s91, 0\n\t"
    "s_add_u32 s90, 1179648, s16\n\t"
    "s_addc_u32 s91, s17, 0\n\t"
    "v_bfe_u32 v233, v0, 3, 1\n\t"
    "v_lshl_add_u32 v26, v233, 11, v3\n\t"
    "v_xor_b32 v27, 64, v26\n\t"
    "s_mul_i32 s75, s72, 3072\n\t"
    "v_add_u32 v40, 65536, v24\n\t"
    "v_add_u32 v41, 65536, v25\n\t"
    "s_waitcnt vmcnt(12)\n\t"
    "v_lshl_add_u64 v[36:37], v[36:37], 4, v[72:73]\n\t"
    "v_lshl_add_u64 v[38:39], v[38:39], 4, v[74:75]\n\t"
    "s_waitcnt lgkmcnt(0)\n\t"
    "s_barrier\n\t"
    "buffer_load_dwordx4 v[120:123], v15, s[12:15], 0 offen nt\n\t"
    "v_add_u32 v28, 65536, v26\n\t"
    "v_add_u32 v29, 65536, v27\n\t"
    "v_add_u32 v30, 131072, v26\n\t"
    "v_add_u32 v31, 131072, v27\n\t"
    "v_add_u32 v32, 9216, v4\n\t"
    "v_add_u32 v33, 18432, v4\n\t"
    "v_add_u32 v34, 27648, v4\n\t"
    "s_mul_i32 s30, s28, 20480\n\t"
    "s_mov_b32 s82, 38912\n\t"
    "s_mov_b32 s83, 0x20000\n\t"
    "v_lshrrev_b32 v233, 3, v0\n\t"
    "v_mul_u32_u24 v233, 1152, v233\n\t"
    "v_xor_b32 v238, v234, v235\n\t"
    "v_lshl_or_b32 v232, v238, 4, v233\n\t"
    "s_waitcnt vmcnt(11)\n\t"
    "s_mov_b64 exec, 0\n\t"
    "s_cmp_eq_u32 s72, 0\n\t"
    "s_cbranch_scc1 L_pg1_%=\n\t"
    "L_bk1_%=:\n\t"
    "s_cmp_eq_u32 s72, 1\n\t"
    "s_cbranch_scc1 L_pg2_%=\n\t"
    "L_bk2_%=:\n\t"
    "s_cmp_eq_u32 s72, 2\n\t"
    "s_cbranch_scc1 L_pg3_%=\n\t"
    "L_bk3_%=:\n\t"
    "s_cmp_eq_u32 s72, 3\n\t"
    "s_cbranch_scc1 L_pg4_%=\n\t"
    "L_bk4_%=:\n\t"
    "s_mov_b64 exec, -1\n\t"
    "v_lshl_add_u64 v[36:37], v[36:37], 4, v[68:69]\n\t"
    "v_lshl_add_u64 v[38:39], v[38:39], 4, v[70:71]\n\t"
    "buffer_load_dwordx4 v[116:119], v14, s[12:15], 0 offen nt\n\t"
    "s_lshl_b32 s73, s34, 5\n\t"
    "s_waitcnt vmcnt(11)\n\t"
    "v_lshl_add_u64 v[36:37], v[36:37], 4, v[64:65]\n\t"
    "v_lshl_add_u64 v[38:39], v[38:39], 4, v[66:67]\n\t"
    "s_mov_b64 vcc, s[58:59]\n\t"
    "v_cndmask_b32_dpp v42, v36, v38, vcc quad_perm:[1,0,3,2] row_mask:0xf bank_mask:0xf\n\t"
    "v_cndmask_b32_dpp v43, v37, v39, vcc quad_perm:[1,0,3,2] row_mask:0xf bank_mask:0xf\n\t"
    "s_mov_b64 vcc, s[56:57]\n\t"
    "v_cndmask_b32_dpp v44, v38, v36, vcc quad_perm:[1,0,3,2] row_mask:0xf bank_mask:0xf\n\t"
    "v_cndmask_b32_dpp v45, v39, v37, vcc quad_perm:[1,0,3,2] row_mask:0xf bank_mask:0xf\n\t"
    "s_mov_b64 vcc, s[60:61]\n\t"
    "v_cndmask_b32_dpp v46, v44, v42, vcc row_shl:4 row_mask:0xf bank_mask:0xf bound_ctrl:1\n\t"
    "v_cndmask_b32_dpp v47, v45, v43, vcc row_shl:4 row_mask:0xf bank_mask:0xf bound_ctrl:1\n\t"
    "v_mov_b32_dpp v44, v42 row_shr:4 row_mask:0xf bank_mask:0xa\n\t"
    "v_mov_b32_dpp v45, v43 row_shr:4 row_mask:0xf bank_mask:0xa\n\t"
    "buffer_store_dwordx4 v[44:47], v250, s[92:95], s73 offen sc1 ; publish first chunk, packed (one workgroup per chunk; out of range elsewhere)\n\t"
    "v_add_u32 v248, 1, v21\n\t"
    "buffer_load_dwordx4 v[112:115], v13, s[12:15], 0 offen nt\n\t"
    "s_add_u32 s29, s70, 128\n\t"
    "s_mul_i32 s29, s29, 1152\n\t"
    "s_sub_u32 s27, s29, 2048\n\t"
    "s_mul_i32 s78, s28, 18432\n\t"
    "s_add_u32 s78, s78, 184320\n\t"
    "s_mov_b32 s79, 0x20000\n\t"
    "s_add_u32 s76, s29, s84\n\t"
    "s_addc_u32 s77, s85, 0\n\t"
    "v_lshlrev_b32 v20, 7, v0\n\t"
    "v_add_u32 v20, 184320, v20\n\t"
    "s_add_u32 s29, s34, 2048\n\t"
    "v_add_u32 v224, s29, v232\n\t"
    "s_add_u32 s29, s35, 1024\n\t"
    "v_add_u32 v225, s29, v232\n\t"
    "s_add_u32 s29, s36, 0\n\t"
    "buffer_load_dwordx4 v[108:111], v12, s[12:15], 0 offen nt\n\t"
    "v_add_u32 v226, s29, v232\n\t"
    "s_add_u32 s29, s37, 2048\n\t"
    "v_add_u32 v227, s29, v232\n\t"
    "s_add_u32 s29, s38, 1024\n\t"
    "v_add_u32 v228, s29, v232\n\t"
    "s_add_u32 s29, s39, 0\n\t"
    "v_add_u32 v229, s29, v232\n\t"
    "s_add_u32 s29, s40, 2048\n\t"
    "v_add_u32 v230, s29, v232\n\t"
    "s_add_u32 s29, s41, 1024\n\t"
    "v_add_u32 v231, s29, v232\n\t"
    "s_add_u32 s29, s42, 0\n\t"
    "v_add_u32 v232, s29, v232\n\t"
    "s_lshl_b32 s29, s72, 5\n\t"
    "buffer_load_dwordx4 v[104:107], v11, s[12:15], 0 offen nt\n\t"
    "v_add_u32 v233, s29, v239\n\t"
    "v_mul_u32_u24 v233, 1152, v233\n\t"
    "v_and_b32 v238, 32, v239\n\t"
    "v_lshlrev_b32 v238, 25, v238\n\t"
    "v_add3_u32 v3, v233, v238, s36\n\t"
    "v_perm_b32 v48, v44, v8, s52\n\t"
    "ds_read_b32 a0, v48\n\t"
    "v_perm_b32 v49, v44, v8, s53\n\t"
    "ds_read_b32 a1, v49\n\t"
    "v_perm_b32 v50, v44, v8, s54\n\t"
    "ds_read_b32 a2, v50\n\t"
    "buffer_load_dwordx4 v[100:103], v10, s[12:15], 0 offen nt\n\t"
    "v_perm_b32 v51, v44, v8, s55\n\t"
    "ds_read_b32 a3, v51\n\t"
    "v_perm_b32 v52, v45, v8, s52\n\t"
    "ds_read_b32 a4, v52\n\t"
    "v_perm_b32 v53, v45, v8, s53\n\t"
    "ds_read_b32 a5, v53\n\t"
    "v_perm_b32 v54, v45, v8, s54\n\t"
    "ds_read_b32 a6, v54\n\t"
    "v_perm_b32 v55, v45, v8, s55\n\t"
    "ds_read_b32 a7, v55\n\t"
    "buffer_load_dwordx4 v[96:99], v1, s[12:15], 0 offen nt\n\t"
    "v_perm_b32 v48, v46, v8, s52\n\t"
    "ds_read_b32 a8, v48\n\t"
    "v_perm_b32 v49, v46, v8, s53\n\t"
    "ds_read_b32 a9, v49\n\t"
    "v_perm_b32 v50, v46, v8, s54\n\t"
    "ds_read_b32 a10, v50\n\t"
    "v_perm_b32 v51, v46, v8, s55\n\t"
    "ds_read_b32 a11, v51\n\t"
    "v_perm_b32 v52, v47, v8, s52\n\t"
    "ds_read_b32 a12, v52\n\t"
    "v_perm_b32 v53, v47, v8, s53\n\t"
    "ds_read_b32 a13, v53\n\t"
    "v_perm_b32 v54, v47, v8, s54\n\t"
    "ds_read_b32 a14, v54\n\t"
    "v_perm_b32 v55, v47, v8, s55\n\t"
    "ds_read_b32 a15, v55\n\t"
    "s_waitcnt vmcnt(13)\n\t"
    "s_barrier\n\t"
    "ds_read_b128 a[144:147], v24 offset:0\n\t"
    "ds_read_b128 a[148:151], v24 offset:2048\n\t"
    "ds_read_b128 a[152:155], v24 offset:4096\n\t"
    "ds_read_b128 a[156:159], v24 offset:6144\n\t"
    "ds_read_b128 a[160:163], v24 offset:8192\n\t"
    "ds_read_b128 a[164:167], v24 offset:10240\n\t"
    "s_waitcnt lgkmcnt(14)\n\t"
    "ds_read_b128 a[168:171], v24 offset:12288\n\t"
    "ds_read_b128 a[172:175], v24 offset:14336\n\t"
    "s_waitcnt lgkmcnt(6)\n\t"
    "v_mfma_f32_16x16x32_bf16 v[128:131], a[144:147], a[0:3], 0\n\t"
    "ds_read_b128 a[176:179], v25 offset:0\n\t"
    "v_mfma_f32_16x16x32_bf16 v[132:135], a[144:147], a[4:7], 0\n\t"
    "v_mfma_f32_16x16x32_bf16 v[136:139], a[148:151], a[0:3], 0\n\t"
    "ds_read_b128 a[180:183], v25 offset:2048\n\t"
    "v_mfma_f32_16x16x32_bf16 v[140:143], a[148:151], a[4:7], 0\n\t"
    "s_waitcnt lgkmcnt(6)\n\t"
    "v_mfma_f32_16x16x32_bf16 v[144:147], a[152:155], a[0:3], 0\n\t"
    "ds_read_b128 a[184:187], v25 offset:4096\n\t"
    "v_mfma_f32_16x16x32_bf16 v[148:151], a[152:155], a[4:7], 0\n\t"
    "v_mfma_f32_16x16x32_bf16 v[152:155], a[156:159], a[0:3], 0\n\t"
    "ds_read_b128 a[188:191], v25 offset:6144\n\t"
    "s_waitcnt lgkmcnt(4)\n\t"
    "v_mfma_f32_16x16x32_bf16 v[156:159], a[156:159], a[4:7], 0\n\t"
    "v_mfma_f32_16x16x32_bf16 v[160:163], a[160:163], a[0:3], 0\n\t"
    "ds_read_b128 a[144:147], v25 offset:8192\n\t"
    "v_mfma_f32_16x16x32_bf16 v[164:167], a[160:163], a[4:7], 0\n\t"
    "v_mfma_f32_16x16x32_bf16 v[168:171], a[164:167], a[0:3], 0\n\t"
    "ds_read_b128 a[148:151], v25 offset:10240\n\t"
    "v_mfma_f32_16x16x32_bf16 v[172:175], a[164:167], a[4:7], 0\n\t"
    "v_mfma_f32_16x16x32_bf16 v[176:179], a[168:171], a[0:3], 0\n\t"
    "ds_read_b128 a[152:155], v25 offset:12288\n\t"
    "s_waitcnt vmcnt(5) ; payload store above has completed (vmcnt retires in order)\n\t"
    "v_mfma_f32_16x16x32_bf16 v[180:183], a[168:171], a[4:7], 0\n\t"
    "buffer_store_dword v248, v6, s[92:95], 0 offen sc1 ; flag := entry value + 1 (publisher only)\n\t"
    "s_add_u32 s8, s35, s84\n\t"
    "s_addc_u32 s9, s85, 0\n\t"
    "v_mfma_f32_16x16x32_bf16 v[184:187], a[172:175], a[0:3], 0\n\t"
    "ds_read_b128 a[156:159], v25 offset:14336\n\t"
    "s_add_u32 m0, s26, 98304\n\t"
    "s_waitcnt vmcnt(10) lgkmcnt(4)\n\t"
    "v_mfma_f32_16x16x32_bf16 v[188:191], a[172:175], a[4:7], 0\n\t"
    "buffer_load_dwordx4 v2, s[8:11], 0 offen offset:0 lds\n\t"
    "s_barrier\n\t"
    "v_mfma_f32_16x16x32_bf16 v[128:131], a[176:179], a[8:11], v[128:131]\n\t"
    "ds_read_b128 a[160:163], v24 offset:16384\n\t"
    "buffer_load_dwordx4 v17, s[8:11], 0 offen offset:1024 lds\n\t"
    "v_mfma_f32_16x16x32_bf16 v[132:135], a[176:179], a[12:15], v[132:135]\n\t"
    "buffer_load_dwordx4 v18, s[8:11], 0 offen offset:2048 lds\n\t"
    "v_mfma_f32_16x16x32_bf16 v[136:139], a[180:183], a[8:11], v[136:139]\n\t"
    "ds_read_b128 a[164:167], v24 offset:18432\n\t"
    "buffer_load_dwordx4 v19, s[8:11], 0 offen offset:3072 lds\n\t"
    "v_mfma_f32_16x16x32_bf16 v[140:143], a[180:183], a[12:15], v[140:143]\n\t"
    "v_mfma_f32_16x16x32_bf16 v[144:147], a[184:187], a[8:11], v[144:147]\n\t"
    "ds_read_b128 a[168:171], v24 offset:20480\n\t"
    "v_mfma_f32_16x16x32_bf16 v[148:151], a[184:187], a[12:15], v[148:151]\n\t"
    "v_mfma_f32_16x16x32_bf16 v[152:155], a[188:191], a[8:11], v[152:155]\n\t"
    "ds_read_b128 a[172:175], v24 offset:22528\n\t"
    "s_waitcnt lgkmcnt(4)\n\t"
    "v_mfma_f32_16x16x32_bf16 v[156:159], a[188:191], a[12:15], v[156:159]\n\t"
    "v_mfma_f32_16x16x32_bf16 v[160:163], a[144:147], a[8:11], v[160:163]\n\t"
    "ds_read_b128 a[176:179], v24 offset:24576\n\t"
    "v_mfma_f32_16x16x32_bf16 v[164:167], a[144:147], a[12:15], v[164:167]\n\t"
    "v_mfma_f32_16x16x32_bf16 v[168:171], a[148:151], a[8:11], v[168:171]\n\t"
    "ds_read_b128 a[180:183], v24 offset:26624\n\t"
    "v_mfma_f32_16x16x32_bf16 v[172:175], a[148:151], a[12:15], v[172:175]\n\t"
    "s_add_u32 s8, s35, s62\n\t"
    "s_addc_u32 s9, s63, 0\n\t"
    "v_mfma_f32_16x16x32_bf16 v[176:179], a[152:155], a[8:11], v[176:179]\n\t"
    "ds_read_b128 a[184:187], v24 offset:28672\n\t"
    "s_add_u32 m0, s26, 114688\n\t"
    "v_mfma_f32_16x16x32_bf16 v[180:183], a[152:155], a[12:15], v[180:183]\n\t"
    "buffer_load_dwordx4 v2, s[8:11], 0 offen offset:0 lds\n\t"
    "v_mfma_f32_16x16x32_bf16 v[184:187], a[156:159], a[8:11], v[184:187]\n\t"
    "ds_read_b128 a[188:191], v24 offset:30720\n\t"
    "s_waitcnt lgkmcnt(4)\n\t"
    "v_mfma_f32_16x16x32_bf16 v[188:191], a[156:159], a[12:15], v[188:191]\n\t"
    "v_mfma_f32_16x16x32_bf16 a[192:195], a[160:163], a[0:3], 0\n\t"
    "ds_read_b128 a[144:147], v25 offset:16384\n\t"
    "buffer_load_dwordx4 v17, s[8:11], 0 offen offset:1024 lds\n\t"
    "v_mfma_f32_16x16x32_bf16 a[196:199], a[160:163], a[4:7], 0\n\t"
    "v_mfma_f32_16x16x32_bf16 a[200:203], a[164:167], a[0:3], 0\n\t"
    "ds_read_b128 a[148:151], v25 offset:18432\n\t"
    "v_mfma_f32_16x16x32_bf16 a[204:207], a[164:167], a[4:7], 0\n\t"
    "buffer_load_dwordx4 v18, s[8:11], 0 offen offset:2048 lds\n\t"
    "v_mfma_f32_16x16x32_bf16 a[208:211], a[168:171], a[0:3], 0\n\t"
    "ds_read_b128 a[152:155], v25 offset:20480\n\t"
    "v_mfma_f32_16x16x32_bf16 a[212:215], a[168:171], a[4:7], 0\n\t"
    "v_lshl_add_u64 v[36:37], v[124:125], 4, v[120:121]\n\t"
    "v_lshl_add_u64 v[38:39], v[126:127], 4, v[122:123]\n\t"
    "v_mfma_f32_16x16x32_bf16 a[216:219], a[172:175], a[0:3], 0\n\t"
    "ds_read_b128 a[156:159], v25 offset:22528\n\t"
    "buffer_load_dwordx4 v19, s[8:11], 0 offen offset:3072 lds\n\t"
    "v_lshl_add_u64 v[36:37], v[36:37], 4, v[116:117]\n\t"
    "v_lshl_add_u64 v[38:39], v[38:39], 4, v[118:119]\n\t"
    "s_waitcnt vmcnt(12) lgkmcnt(4)\n\t"
    "v_mfma_f32_16x16x32_bf16 a[220:223], a[172:175], a[4:7], 0\n\t"
    "v_lshl_add_u64 v[36:37], v[36:37], 4, v[112:113]\n\t"
    "v_lshl_add_u64 v[38:39], v[38:39], 4, v[114:115]\n\t"
    "v_mfma_f32_16x16x32_bf16 a[224:227], a[176:179], a[0:3], 0\n\t"
    "ds_read_b128 a[160:163], v25 offset:24576\n\t"
    "v_lshl_add_u64 v[36:37], v[36:37], 4, v[108:109]\n\t"
    "v_lshl_add_u64 v[38:39], v[38:39], 4, v[110:111]\n\t"
    "s_waitcnt vmcnt(10)\n\t"
    "v_mfma_f32_16x16x32_bf16 a[228:231], a[176:179], a[4:7], 0\n\t"
    "v_lshl_add_u64 v[36:37], v[36:37], 4, v[104:105]\n\t"
    "v_lshl_add_u64 v[38:39], v[38:39], 4, v[106:107]\n\t"
    "s_add_u32 s8, s36, s84\n\t"
    "s_addc_u32 s9, s85, 0\n\t"
    "v_mfma_f32_16x16x32_bf16 a[232:235], a[180:183], a[0:3], 0\n\t"
    "ds_read_b128 a[164:167], v25 offset:26624\n\t"
    "v_lshl_add_u64 v[36:37], v[36:37], 4, v[100:101]\n\t"
    "v_lshl_add_u64 v[38:39], v[38:39], 4, v[102:103]\n\t"
    "s_add_u32 m0, s26, 131072\n\t"
    "s_waitcnt vmcnt(9)\n\t"
    "v_mfma_f32_16x16x32_bf16 a[236:239], a[180:183], a[4:7], 0\n\t"
    "buffer_load_dwordx4 v2, s[8:11], 0 offen offset:0 lds\n\t"
    "v_lshl_add_u64 v[36:37], v[36:37], 4, v[96:97]\n\t"
    "v_lshl_add_u64 v[38:39], v[38:39], 4, v[98:99]\n\t"
    "s_lshl_b32 s73, s35, 5\n\t"
    "v_mfma_f32_16x16x32_bf16 a[240:243], a[184:187], a[0:3], 0\n\t"
    "ds_read_b128 a[168:171], v25 offset:28672\n\t"
    "s_mov_b64 vcc, s[58:59]\n\t"
    "v_cndmask_b32_dpp v42, v36, v38, vcc quad_perm:[1,0,3,2] row_mask:0xf bank_mask:0xf\n\t"
    "v_mfma_f32_16x16x32_bf16 a[244:247], a[184:187], a[4:7], 0\n\t"
    "v_cndmask_b32_dpp v43, v37, v39, vcc quad_perm:[1,0,3,2] row_mask:0xf bank_mask:0xf\n\t"
    "v_mfma_f32_16x16x32_bf16 a[248:251], a[188:191], a[0:3], 0\n\t"
    "ds_read_b128 a[172:175], v25 offset:30720\n\t"
    "s_mov_b64 vcc, s[56:57]\n\t"
    "v_cndmask_b32_dpp v192, v38, v36, vcc quad_perm:[1,0,3,2] row_mask:0xf bank_mask:0xf\n\t"
    "s_waitcnt lgkmcnt(4)\n\t"
    "v_mfma_f32_16x16x32_bf16 a[252:255], a[188:191], a[4:7], 0\n\t"
    "v_cndmask_b32_dpp v193, v39, v37, vcc quad_perm:[1,0,3,2] row_mask:0xf bank_mask:0xf\n\t"
    "v_mfma_f32_16x16x32_bf16 a[192:195], a[144:147], a[8:11], a[192:195]\n\t"
    "buffer_load_dwordx4 v17, s[8:11], 0 offen offset:1024 lds\n\t"
    "s_mov_b64 vcc, s[60:61]\n\t"
    "v_mfma_f32_16x16x32_bf16 a[196:199], a[144:147], a[12:15], a[196:199]\n\t"
    "v_cndmask_b32_dpp v194, v192, v42, vcc row_shl:4 row_mask:0xf bank_mask:0xf bound_ctrl:1\n\t"
    "v_cndmask_b32_dpp v195, v193, v43, vcc row_shl:4 row_mask:0xf bank_mask:0xf bound_ctrl:1\n\t"
    "v_mfma_f32_16x16x32_bf16 a[200:203], a[148:151], a[8:11], a[200:203]\n\t"
    "v_mov_b32_dpp v192, v42 row_shr:4 row_mask:0xf bank_mask:0xa\n\t"
    "v_mfma_f32_16x16x32_bf16 a[204:207], a[148:151], a[12:15], a[204:207]\n\t"
    "v_mov_b32_dpp v193, v43 row_shr:4 row_mask:0xf bank_mask:0xa\n\t"
    "v_mfma_f32_16x16x32_bf16 a[208:211], a[152:155], a[8:11], a[208:211]\n\t"
    "buffer_store_dwordx4 v[192:195], v246, s[92:95], s73 offen sc1 ; publish second chunk (one workgroup per chunk; out of range elsewhere)\n\t"
    "v_mfma_f32_16x16x32_bf16 a[212:215], a[152:155], a[12:15], a[212:215]\n\t"
    "buffer_load_dwordx4 v18, s[8:11], 0 offen offset:2048 lds\n\t"
    "v_perm_b32 v48, v192, v8, s52\n\t"
    "v_mfma_f32_16x16x32_bf16 a[216:219], a[156:159], a[8:11], a[216:219]\n\t"
    "ds_read_b32 a16, v48\n\t"
    "v_perm_b32 v49, v192, v8, s53\n\t"
    "s_waitcnt lgkmcnt(1)\n\t"
    "v_mfma_f32_16x16x32_bf16 a[220:223], a[156:159], a[12:15], a[220:223]\n\t"
    "ds_read_b32 a17, v49\n\t"
    "v_perm_b32 v50, v192, v8, s54\n\t"
    "v_perm_b32 v51, v192, v8, s55\n\t"
    "v_mfma_f32_16x16x32_bf16 a[224:227], a[160:163], a[8:11], a[224:227]\n\t"
    "ds_read_b32 a18, v50\n\t"
    "ds_read_b32 a19, v51\n\t"
    "v_perm_b32 v52, v193, v8, s52\n\t"
    "v_perm_b32 v53, v193, v8, s53\n\t"
    "s_waitcnt vmcnt(8)\n\t"
    "v_mfma_f32_16x16x32_bf16 a[228:231], a[160:163], a[12:15], a[228:231]\n\t"
    "ds_read_b32 a20, v52\n\t"
    "ds_read_b32 a21, v53\n\t"
    "v_perm_b32 v54, v193, v8, s54\n\t"
    "v_perm_b32 v55, v193, v8, s55\n\t"
    "s_barrier\n\t"
    "v_mfma_f32_16x16x32_bf16 a[232:235], a[164:167], a[8:11], a[232:235]\n\t"
    "ds_read_b128 a[176:179], v24 offset:32768\n\t"
    "ds_read_b128 a[180:183], v24 offset:34816\n\t"
    "buffer_load_dwordx4 v19, s[8:11], 0 offen offset:3072 lds\n\t"
    "buffer_load_dword v22, v23, s[92:95], 0 offen sc1 ; poll\n\t"
    "ds_read_b32 a22, v54\n\t"
    "ds_read_b32 a23, v55\n\t"
    "v_perm_b32 v48, v194, v8, s52\n\t"
    "v_mfma_f32_16x16x32_bf16 a[236:239], a[164:167], a[12:15], a[236:239]\n\t"
    "ds_read_b128 a[184:187], v24 offset:36864\n\t"
    "ds_read_b128 a[188:191], v24 offset:38912\n\t"
    "v_perm_b32 v49, v194, v8, s53\n\t"
    "v_mfma_f32_16x16x32_bf16 a[240:243], a[168:171], a[8:11], a[240:243]\n\t"
    "ds_read_b128 a[144:147], v24 offset:40960\n\t"
    "ds_read_b128 a[148:151], v24 offset:43008\n\t"
    "v_perm_b32 v50, v194, v8, s54\n\t"
    "v_mfma_f32_16x16x32_bf16 a[244:247], a[168:171], a[12:15], a[244:247]\n\t"
    "ds_read_b128 a[152:155], v24 offset:45056\n\t"
    "s_add_u32 s8, s37, s84\n\t"
    "s_addc_u32 s9, s85, 0\n\t"
    "v_mfma_f32_16x16x32_bf16 a[248:251], a[172:175], a[8:11], a[248:251]\n\t"
    "ds_read_b128 a[156:159], v24 offset:47104\n\t"
    "s_add_u32 m0, s26, 147456\n\t"
    "s_waitcnt lgkmcnt(6)\n\t"
    "v_mfma_f32_16x16x32_bf16 a[252:255], a[172:175], a[12:15], a[252:255]\n\t"
    "buffer_load_dwordx4 v2, s[8:11], 0 offen offset:0 lds\n\t"
    "buffer_load_dword v35, v5, s[20:23], 0 offen\n\t"
    "L_pg1_%=:\n\t"
    "s_cbranch_execz L_bk1_%=\n\t"
    "v_mfma_f32_16x16x32_bf16 v[128:131], a[176:179], a[16:19], v[128:131]\n\t"
    "ds_read_b128 a[160:163], v25 offset:32768\n\t"
    "v_perm_b32 v51, v194, v8, s55\n\t"
    "v_mfma_f32_16x16x32_bf16 v[132:135], a[176:179], a[20:23], v[132:135]\n\t"
    "ds_read_b32 a24, v48\n\t"
    "v_perm_b32 v52, v195, v8, s52\n\t"
    "v_mfma_f32_16x16x32_bf16 v[136:139], a[180:183], a[16:19], v[136:139]\n\t"
    "ds_read_b128 a[164:167], v25 offset:34816\n\t"
    "v_perm_b32 v53, v195, v8, s53\n\t"
    "v_mfma_f32_16x16x32_bf16 v[140:143], a[180:183], a[20:23], v[140:143]\n\t"
    "ds_read_b32 a25, v49\n\t"
    "s_waitcnt lgkmcnt(8)\n\t"
    "v_mfma_f32_16x16x32_bf16 v[144:147], a[184:187], a[16:19], v[144:147]\n\t"
    "ds_read_b128 a[168:171], v25 offset:36864\n\t"
    "buffer_load_dwordx4 v17, s[8:11], 0 offen offset:1024 lds\n\t"
    "v_perm_b32 v54, v195, v8, s54\n\t"
    "v_mfma_f32_16x16x32_bf16 v[148:151], a[184:187], a[20:23], v[148:151]\n\t"
    "ds_read_b32 a26, v50\n\t"
    "v_perm_b32 v55, v195, v8, s55\n\t"
    "v_mfma_f32_16x16x32_bf16 v[152:155], a[188:191], a[16:19], v[152:155]\n\t"
    "ds_read_b128 a[172:175], v25 offset:38912\n\t"
    "ds_read_b32 a27, v51\n\t"
    "s_waitcnt lgkmcnt(8)\n\t"
    "v_mfma_f32_16x16x32_bf16 v[156:159], a[188:191], a[20:23], v[156:159]\n\t"
    "ds_read_b32 a28, v52\n\t"
    "v_mfma_f32_16x16x32_bf16 v[160:163], a[144:147], a[16:19], v[160:163]\n\t"
    "ds_read_b128 a[176:179], v25 offset:40960\n\t"
    "ds_read_b32 a29, v53\n\t"
    "v_mfma_f32_16x16x32_bf16 v[164:167], a[144:147], a[20:23], v[164:167]\n\t"
    "buffer_load_dwordx4 v18, s[8:11], 0 offen offset:2048 lds\n\t"
    "ds_read_b32 a30, v54\n\t"
    "v_mfma_f32_16x16x32_bf16 v[168:171], a[148:151], a[16:19], v[168:171]\n\t"
    "ds_read_b128 a[180:183], v25 offset:43008\n\t"
    "ds_read_b32 a31, v55\n\t"
    "v_mfma_f32_16x16x32_bf16 v[172:175], a[148:151], a[20:23], v[172:175]\n\t"
    "v_mfma_f32_16x16x32_bf16 v[176:179], a[152:155], a[16:19], v[176:179]\n\t"
    "ds_read_b128 a[184:187], v25 offset:45056\n\t"
    "v_mfma_f32_16x16x32_bf16 v[180:183], a[152:155], a[20:23], v[180:183]\n\t"
    "v_mfma_f32_16x16x32_bf16 v[184:187], a[156:159], a[16:19], v[184:187]\n\t"
    "ds_read_b128 a[188:191], v25 offset:47104\n\t"
    "buffer_load_dwordx4 v19, s[8:11], 0 offen offset:3072 lds\n\t"
    "s_waitcnt lgkmcnt(2)\n\t"
    "v_mfma_f32_16x16x32_bf16 v[188:191], a[156:159], a[20:23], v[188:191]\n\t"
    "v_mfma_f32_16x16x32_bf16 v[128:131], a[160:163], a[24:27], v[128:131]\n\t"
    "v_mfma_f32_16x16x32_bf16 v[132:135], a[160:163], a[28:31], v[132:135]\n\t"
    "s_add_u32 s8, s38, s84\n\t"
    "s_addc_u32 s9, s85, 0\n\t"
    "v_mfma_f32_16x16x32_bf16 v[136:139], a[164:167], a[24:27], v[136:139]\n\t"
    "s_add_u32 m0, s26, 65536\n\t"
    "v_mfma_f32_16x16x32_bf16 v[140:143], a[164:167], a[28:31], v[140:143]\n\t"
    "buffer_load_dwordx4 v2, s[8:11], 0 offen offset:0 lds\n\t"
    "v_mfma_f32_16x16x32_bf16 v[144:147], a[168:171], a[24:27], v[144:147]\n\t"
    "v_mfma_f32_16x16x32_bf16 v[148:151], a[168:171], a[28:31], v[148:151]\n\t"
    "v_mfma_f32_16x16x32_bf16 v[152:155], a[172:175], a[24:27], v[152:155]\n\t"
    "s_waitcnt lgkmcnt(0)\n\t"
    "s_waitcnt vmcnt(6)\n\t"
    "L_chk0_%=:\n\t"
    "v_cmp_ne_u32 s[96:97], v21, v22 ; which flags have changed since entry\n\t"
    "s_andn2_b32 s29, s31, s96\n\t"
    "s_cbranch_scc1 L_spin0_%= ; not all yet: poll again (bounded)\n\t"
    "L_join0_%=:\n\t"
    "v_mfma_f32_16x16x32_bf16 v[156:159], a[172:175], a[28:31], v[156:159]\n\t"
    "buffer_load_dwordx4 v[196:199], v63, s[92:95], s45 offen sc1 ; packed chunk\n\t"
    "v_mfma_f32_16x16x32_bf16 v[160:163], a[176:179], a[24:27], v[160:163]\n\t"
    "buffer_load_dwordx4 v17, s[8:11], 0 offen offset:1024 lds\n\t"
    "v_mfma_f32_16x16x32_bf16 v[164:167], a[176:179], a[28:31], v[164:167]\n\t"
    "v_mfma_f32_16x16x32_bf16 v[168:171], a[180:183], a[24:27], v[168:171]\n\t"
    "v_mfma_f32_16x16x32_bf16 v[172:175], a[180:183], a[28:31], v[172:175]\n\t"
    "buffer_load_dwordx4 v[200:203], v63, s[92:95], s46 offen sc1 ; packed chunk\n\t"
    "s_barrier\n\t"
    "v_mfma_f32_16x16x32_bf16 v[176:179], a[184:187], a[24:27], v[176:179]\n\t"
    "ds_read_b128 a[144:147], v24 offset:49152\n\t"
    "ds_read_b128 a[148:151], v24 offset:51200\n\t"
    "v_mfma_f32_16x16x32_bf16 v[180:183], a[184:187], a[28:31], v[180:183]\n\t"
    "ds_read_b128 a[152:155], v24 offset:53248\n\t"
    "ds_read_b128 a[156:159], v24 offset:55296\n\t"
    "buffer_load_dwordx4 v18, s[8:11], 0 offen offset:2048 lds\n\t"
    "v_mfma_f32_16x16x32_bf16 v[184:187], a[188:191], a[24:27], v[184:187]\n\t"
    "ds_read_b128 a[160:163], v24 offset:57344\n\t"
    "ds_read_b128 a[164:167], v24 offset:59392\n\t"
    "s_waitcnt vmcnt(13) ; second payload store has completed\n\t"
    "v_mfma_f32_16x16x32_bf16 v[188:191], a[188:191], a[28:31], v[188:191]\n\t"
    "ds_read_b128 a[168:171], v24 offset:61440\n\t"
    "ds_read_b128 a[172:175], v24 offset:63488\n\t"
    "buffer_store_dword v248, v247, s[92:95], 0 offen sc1 ; second flag := entry value + 1 (publisher only)\n\t"
    "buffer_load_dwordx4 v[204:207], v63, s[92:95], s47 offen sc1 ; packed chunk\n\t"
    "s_waitcnt lgkmcnt(6)\n\t"
    "v_mfma_f32_16x16x32_bf16 a[192:195], a[144:147], a[16:19], a[192:195]\n\t"
    "ds_read_b128 a[176:179], v25 offset:49152\n\t"
    "v_mfma_f32_16x16x32_bf16 a[196:199], a[144:147], a[20:23], a[196:199]\n\t"
    "v_mfma_f32_16x16x32_bf16 a[200:203], a[148:151], a[16:19], a[200:203]\n\t"
    "ds_read_b128 a[180:183], v25 offset:51200\n\t"
    "buffer_load_dwordx4 v19, s[8:11], 0 offen offset:3072 lds\n\t"
    "v_mfma_f32_16x16x32_bf16 a[204:207], a[148:151], a[20:23], a[204:207]\n\t"
    "buffer_load_dwordx4 v[208:211], v63, s[92:95], s48 offen sc1 ; packed chunk\n\t"
    "s_waitcnt lgkmcnt(6)\n\t"
    "v_mfma_f32_16x16x32_bf16 a[208:211], a[152:155], a[16:19], a[208:211]\n\t"
    "ds_read_b128 a[184:187], v25 offset:53248\n\t"
    "v_mfma_f32_16x16x32_bf16 a[212:215], a[152:155], a[20:23], a[212:215]\n\t"
    "s_add_u32 s8, s39, s84\n\t"
    "s_addc_u32 s9, s85, 0\n\t"
    "v_mfma_f32_16x16x32_bf16 a[216:219], a[156:159], a[16:19], a[216:219]\n\t"
    "ds_read_b128 a[188:191], v25 offset:55296\n\t"
    "s_add_u32 m0, s26, 81920\n\t"
    "s_waitcnt lgkmcnt(4)\n\t"
    "v_mfma_f32_16x16x32_bf16 a[220:223], a[156:159], a[20:23], a[220:223]\n\t"
    "buffer_load_dwordx4 v2, s[8:11], 0 offen offset:0 lds\n\t"
    "v_mfma_f32_16x16x32_bf16 a[224:227], a[160:163], a[16:19], a[224:227]\n\t"
    "ds_read_b128 a[144:147], v25 offset:57344\n\t"
    "v_mfma_f32_16x16x32_bf16 a[228:231], a[160:163], a[20:23], a[228:231]\n\t"
    "v_mfma_f32_16x16x32_bf16 a[232:235], a[164:167], a[16:19], a[232:235]\n\t"
    "ds_read_b128 a[148:151], v25 offset:59392\n\t"
    "v_mfma_f32_16x16x32_bf16 a[236:239], a[164:167], a[20:23], a[236:239]\n\t"
    "v_mfma_f32_16x16x32_bf16 a[240:243], a[168:171], a[16:19], a[240:243]\n\t"
    "ds_read_b128 a[152:155], v25 offset:61440\n\t"
    "buffer_load_dwordx4 v17, s[8:11], 0 offen offset:1024 lds\n\t"
    "v_mfma_f32_16x16x32_bf16 a[244:247], a[168:171], a[20:23], a[244:247]\n\t"
    "v_mfma_f32_16x16x32_bf16 a[248:251], a[172:175], a[16:19], a[248:251]\n\t"
    "ds_read_b128 a[156:159], v25 offset:63488\n\t"
    "s_waitcnt vmcnt(17) lgkmcnt(4)\n\t"
    "v_mfma_f32_16x16x32_bf16 a[252:255], a[172:175], a[20:23], a[252:255]\n\t"
    "s_barrier\n\t"
    "v_mfma_f32_16x16x32_bf16 a[192:195], a[176:179], a[24:27], a[192:195]\n\t"
    "ds_read_b128 a[160:163], v40 offset:0\n\t"
    "v_mfma_f32_16x16x32_bf16 a[196:199], a[176:179], a[28:31], a[196:199]\n\t"
    "buffer_load_dwordx4 v18, s[8:11], 0 offen offset:2048 lds\n\t"
    "s_waitcnt vmcnt(10)\n\t"
    "v_mfma_f32_16x16x32_bf16 a[200:203], a[180:183], a[24:27], a[200:203]\n\t"
    "ds_read_b128 a[164:167], v40 offset:2048\n\t"
    "v_perm_b32 v48, v196, v8, s52\n\t"
    "v_mfma_f32_16x16x32_bf16 a[204:207], a[180:183], a[28:31], a[204:207]\n\t"
    "ds_read_b32 a32, v48\n\t"
    "v_perm_b32 v49, v196, v8, s53\n\t"
    "v_mfma_f32_16x16x32_bf16 a[208:211], a[184:187], a[24:27], a[208:211]\n\t"
    "ds_read_b128 a[168:171], v40 offset:4096\n\t"
    "ds_read_b32 a33, v49\n\t"
    "v_perm_b32 v50, v196, v8, s54\n\t"
    "v_mfma_f32_16x16x32_bf16 a[212:215], a[184:187], a[28:31], a[212:215]\n\t"
    "ds_read_b32 a34, v50\n\t"
    "v_perm_b32 v51, v196, v8, s55\n\t"
    "v_mfma_f32_16x16x32_bf16 a[216:219], a[188:191], a[24:27], a[216:219]\n\t"
    "ds_read_b128 a[172:175], v40 offset:6144\n\t"
    "buffer_load_dwordx4 v19, s[8:11], 0 offen offset:3072 lds\n\t"
    "ds_read_b32 a35, v51\n\t"
    "v_perm_b32 v52, v197, v8, s52\n\t"
    "s_waitcnt lgkmcnt(8)\n\t"
    "v_mfma_f32_16x16x32_bf16 a[220:223], a[188:191], a[28:31], a[220:223]\n\t"
    "ds_read_b32 a36, v52\n\t"
    "v_perm_b32 v53, v197, v8, s53\n\t"
    "v_mfma_f32_16x16x32_bf16 a[224:227], a[144:147], a[24:27], a[224:227]\n\t"
    "ds_read_b128 a[176:179], v40 offset:8192\n\t"
    "ds_read_b32 a37, v53\n\t"
    "v_perm_b32 v54, v197, v8, s54\n\t"
    "v_mfma_f32_16x16x32_bf16 a[228:231], a[144:147], a[28:31], a[228:231]\n\t"
    "ds_read_b32 a38, v54\n\t"
    "s_add_u32 s8, s40, s84\n\t"
    "s_addc_u32 s9, s85, 0\n\t"
    "v_perm_b32 v55, v197, v8, s55\n\t"
    "v_mfma_f32_16x16x32_bf16 a[232:235], a[148:151], a[24:27], a[232:235]\n\t"
    "ds_read_b128 a[180:183], v40 offset:10240\n\t"
    "ds_read_b32 a39, v55\n\t"
    "s_add_u32 m0, s26, 98304\n\t"
    "v_mfma_f32_16x16x32_bf16 a[236:239], a[148:151], a[28:31], a[236:239]\n\t"
    "buffer_load_dwordx4 v2, s[8:11], 0 offen offset:0 lds\n\t"
    "s_waitcnt vmcnt(10)\n\t"
    "v_mfma_f32_16x16x32_bf16 a[240:243], a[152:155], a[24:27], a[240:243]\n\t"
    "ds_read_b128 a[184:187], v40 offset:12288\n\t"
    "v_perm_b32 v48, v198, v8, s52\n\t"
    "v_mfma_f32_16x16x32_bf16 a[244:247], a[152:155], a[28:31], a[244:247]\n\t"
    "ds_read_b32 a40, v48\n\t"
    "v_perm_b32 v49, v198, v8, s53\n\t"
    "v_mfma_f32_16x16x32_bf16 a[248:251], a[156:159], a[24:27], a[248:251]\n\t"
    "ds_read_b128 a[188:191], v40 offset:14336\n\t"
    "s_waitcnt lgkmcnt(3)\n\t"
    "v_mfma_f32_16x16x32_bf16 a[252:255], a[156:159], a[28:31], a[252:255]\n\t"
    "ds_read_b32 a41, v49\n\t"
    "v_perm_b32 v50, v198, v8, s54\n\t"
    "v_mfma_f32_16x16x32_bf16 v[128:131], a[160:163], a[32:35], v[128:131]\n\t"
    "ds_read_b128 a[144:147], v41 offset:0\n\t"
    "buffer_load_dwordx4 v17, s[8:11], 0 offen offset:1024 lds\n\t"
    "v_perm_b32 v51, v198, v8, s55\n\t"
    "v_mfma_f32_16x16x32_bf16 v[132:135], a[160:163], a[36:39], v[132:135]\n\t"
    "ds_read_b32 a42, v50\n\t"
    "v_perm_b32 v52, v199, v8, s52\n\t"
    "v_mfma_f32_16x16x32_bf16 v[136:139], a[164:167], a[32:35], v[136:139]\n\t"
    "ds_read_b128 a[148:151], v41 offset:2048\n\t"
    "v_perm_b32 v53, v199, v8, s53\n\t"
    "v_mfma_f32_16x16x32_bf16 v[140:143], a[164:167], a[36:39], v[140:143]\n\t"
    "ds_read_b32 a43, v51\n\t"
    "v_perm_b32 v54, v199, v8, s54\n\t"
    "v_mfma_f32_16x16x32_bf16 v[144:147], a[168:171], a[32:35], v[144:147]\n\t"
    "ds_read_b128 a[152:155], v41 offset:4096\n\t"
    "v_perm_b32 v55, v199, v8, s55\n\t"
    "v_mfma_f32_16x16x32_bf16 v[148:151], a[168:171], a[36:39], v[148:151]\n\t"
    "buffer_load_dwordx4 v18, s[8:11], 0 offen offset:2048 lds\n\t"
    "v_perm_b32 v48, v200, v8, s52\n\t"
    "v_mfma_f32_16x16x32_bf16 v[152:155], a[172:175], a[32:35], v[152:155]\n\t"
    "ds_read_b128 a[156:159], v41 offset:6144\n\t"
    "s_waitcnt lgkmcnt(7)\n\t"
    "v_mfma_f32_16x16x32_bf16 v[156:159], a[172:175], a[36:39], v[156:159]\n\t"
    "ds_read_b32 a44, v52\n\t"
    "v_perm_b32 v49, v200, v8, s53\n\t"
    "v_mfma_f32_16x16x32_bf16 v[160:163], a[176:179], a[32:35], v[160:163]\n\t"
    "ds_read_b128 a[160:163], v41 offset:8192\n\t"
    "ds_read_b32 a45, v53\n\t"
    "v_perm_b32 v50, v200, v8, s54\n\t"
    "v_mfma_f32_16x16x32_bf16 v[164:167], a[176:179], a[36:39], v[164:167]\n\t"
    "ds_read_b32 a46, v54\n\t"
    "v_perm_b32 v51, v200, v8, s55\n\t"
    "v_mfma_f32_16x16x32_bf16 v[168:171], a[180:183], a[32:35], v[168:171]\n\t"
    "ds_read_b128 a[164:167], v41 offset:10240\n\t"
    "buffer_load_dwordx4 v19, s[8:11], 0 offen offset:3072 lds\n\t"
    "ds_read_b32 a47, v55\n\t"
    "v_perm_b32 v52, v201, v8, s52\n\t"
    "v_mfma_f32_16x16x32_bf16 v[172:175], a[180:183], a[36:39], v[172:175]\n\t"
    "ds_read_b32 a48, v48\n\t"
    "v_perm_b32 v53, v201, v8, s53\n\t"
    "v_mfma_f32_16x16x32_bf16 v[176:179], a[184:187], a[32:35], v[176:179]\n\t"
    "ds_read_b128 a[168:171], v41 offset:12288\n\t"
    "v_perm_b32 v54, v201, v8, s54\n\t"
    "v_mfma_f32_16x16x32_bf16 v[180:183], a[184:187], a[36:39], v[180:183]\n\t"
    "ds_read_b32 a49, v49\n\t"
    "v_perm_b32 v55, v201, v8, s55\n\t"
    "v_mfma_f32_16x16x32_bf16 v[184:187], a[188:191], a[32:35], v[184:187]\n\t"
    "ds_read_b128 a[172:175], v41 offset:14336\n\t"
    "s_waitcnt vmcnt(9) lgkmcnt(4)\n\t"
    "v_mfma_f32_16x16x32_bf16 v[188:191], a[188:191], a[36:39], v[188:191]\n\t"
    "ds_read_b32 a50, v50\n\t"
    "s_barrier\n\t"
    "v_mfma_f32_16x16x32_bf16 v[128:131], a[144:147], a[40:43], v[128:131]\n\t"
    "ds_read_b128 a[176:179], v40 offset:16384\n\t"
    "v_perm_b32 v48, v202, v8, s52\n\t"
    "v_mfma_f32_16x16x32_bf16 v[132:135], a[144:147], a[44:47], v[132:135]\n\t"
    "ds_read_b32 a51, v51\n\t"
    "s_add_u32 s8, s41, s84\n\t"
    "s_addc_u32 s9, s85, 0\n\t"
    "v_mfma_f32_16x16x32_bf16 v[136:139], a[148:151], a[40:43], v[136:139]\n\t"
    "ds_read_b128 a[180:183], v40 offset:18432\n\t"
    "s_add_u32 m0, s26, 114688\n\t"
    "v_mfma_f32_16x16x32_bf16 v[140:143], a[148:151], a[44:47], v[140:143]\n\t"
    "buffer_load_dwordx4 v2, s[8:11], 0 offen offset:0 lds\n\t"
    "v_perm_b32 v49, v202, v8, s53\n\t"
    "v_mfma_f32_16x16x32_bf16 v[144:147], a[152:155], a[40:43], v[144:147]\n\t"
    "ds_read_b128 a[184:187], v40 offset:20480\n\t"
    "v_perm_b32 v50, v202, v8, s54\n\t"
    "v_mfma_f32_16x16x32_bf16 v[148:151], a[152:155], a[44:47], v[148:151]\n\t"
    "ds_read_b32 a52, v52\n\t"
    "v_perm_b32 v51, v202, v8, s55\n\t"
    "v_mfma_f32_16x16x32_bf16 v[152:155], a[156:159], a[40:43], v[152:155]\n\t"
    "ds_read_b128 a[188:191], v40 offset:22528\n\t"
    "s_waitcnt lgkmcnt(7)\n\t"
    "v_mfma_f32_16x16x32_bf16 v[156:159], a[156:159], a[44:47], v[156:159]\n\t"
    "ds_read_b32 a53, v53\n\t"
    "v_perm_b32 v52, v203, v8, s52\n\t"
    "v_mfma_f32_16x16x32_bf16 v[160:163], a[160:163], a[40:43], v[160:163]\n\t"
    "ds_read_b128 a[144:147], v40 offset:24576\n\t"
    "buffer_load_dwordx4 v17, s[8:11], 0 offen offset:1024 lds\n\t"
    "v_perm_b32 v53, v203, v8, s53\n\t"
    "v_mfma_f32_16x16x32_bf16 v[164:167], a[160:163], a[44:47], v[164:167]\n\t"
    "ds_read_b32 a54, v54\n\t"
    "v_perm_b32 v54, v203, v8, s54\n\t"
    "v_mfma_f32_16x16x32_bf16 v[168:171], a[164:167], a[40:43], v[168:171]\n\t"
    "ds_read_b128 a[148:151], v40 offset:26624\n\t"
    "ds_read_b32 a55, v55\n\t"
    "v_perm_b32 v55, v203, v8, s55\n\t"
    "v_mfma_f32_16x16x32_bf16 v[172:175], a[164:167], a[44:47], v[172:175]\n\t"
    "ds_read_b32 a56, v48\n\t"
    "v_mfma_f32_16x16x32_bf16 v[176:179], a[168:171], a[40:43], v[176:179]\n\t"
    "ds_read_b128 a[152:155], v40 offset:28672\n\t"
    "buffer_load_dword v22, v23, s[92:95], 0 offen sc1 ; poll\n\t"
    "v_perm_b32 v48, v204, v8, s52\n\t"
    "v_mfma_f32_16x16x32_bf16 v[180:183], a[168:171], a[44:47], v[180:183]\n\t"
    "buffer_load_dwordx4 v18, s[8:11], 0 offen offset:2048 lds\n\t"
    "v_accvgpr_read_b32 v64, a192\n\t"
    "v_mfma_f32_16x16x32_bf16 v[184:187], a[172:175], a[40:43], v[184:187]\n\t"
    "ds_read_b128 a[156:159], v40 offset:30720\n\t"
    "s_waitcnt lgkmcnt(3)\n\t"
    "v_mfma_f32_16x16x32_bf16 v[188:191], a[172:175], a[44:47], v[188:191]\n\t"
    "ds_read_b32 a57, v49\n\t"
    "v_perm_b32 v49, v204, v8, s53\n\t"
    "v_mfma_f32_16x16x32_bf16 v[128:131], a[176:179], a[48:51], v[128:131]\n\t"
    "ds_read_b128 a[160:163], v41 offset:16384\n\t"
    "v_accvgpr_read_b32 v65, a193\n\t"
    "v_mfma_f32_16x16x32_bf16 v[132:135], a[176:179], a[52:55], v[132:135]\n\t"
    "ds_read_b32 a58, v50\n\t"
    "v_perm_b32 v50, v204, v8, s54\n\t"
    "v_mfma_f32_16x16x32_bf16 v[136:139], a[180:183], a[48:51], v[136:139]\n\t"
    "ds_read_b128 a[164:167], v41 offset:18432\n\t"
    "buffer_load_dwordx4 v19, s[8:11], 0 offen offset:3072 lds\n\t"
    "v_accvgpr_read_b32 v66, a194\n\t"
    "v_mfma_f32_16x16x32_bf16 v[140:143], a[180:183], a[52:55], v[140:143]\n\t"
    "ds_read_b32 a59, v51\n\t"
    "v_perm_b32 v51, v204, v8, s55\n\t"
    "v_mfma_f32_16x16x32_bf16 v[144:147], a[184:187], a[48:51], v[144:147]\n\t"
    "ds_read_b128 a[168:171], v41 offset:20480\n\t"
    "v_accvgpr_read_b32 v67, a195\n\t"
    "v_mfma_f32_16x16x32_bf16 v[148:151], a[184:187], a[52:55], v[148:151]\n\t"
    "ds_read_b32 a60, v52\n\t"
    "v_perm_b32 v52, v205, v8, s52\n\t"
    "v_mfma_f32_16x16x32_bf16 v[152:155], a[188:191], a[48:51], v[152:155]\n\t"
    "ds_read_b128 a[172:175], v41 offset:22528\n\t"
    "s_waitcnt lgkmcnt(8)\n\t"
    "v_mfma_f32_16x16x32_bf16 v[156:159], a[188:191], a[52:55], v[156:159]\n\t"
    "ds_read_b32 a61, v53\n\t"
    "v_perm_b32 v53, v205, v8, s53\n\t"
    "v_mfma_f32_16x16x32_bf16 v[160:163], a[144:147], a[48:51], v[160:163]\n\t"
    "ds_read_b128 a[176:179], v41 offset:24576\n\t"
    "v_accvgpr_read_b32 v68, a196\n\t"
    "v_mfma_f32_16x16x32_bf16 v[164:167], a[144:147], a[52:55], v[164:167]\n\t"
    "ds_read_b32 a62, v54\n\t"
    "v_perm_b32 v54, v205, v8, s54\n\t"
    "v_mfma_f32_16x16x32_bf16 v[168:171], a[148:151], a[48:51], v[168:171]\n\t"
    "ds_read_b128 a[180:183], v41 offset:26624\n\t"
    "ds_read_b32 a63, v55\n\t"
    "v_perm_b32 v55, v205, v8, s55\n\t"
    "v_mfma_f32_16x16x32_bf16 v[172:175], a[148:151], a[52:55], v[172:175]\n\t"
    "ds_read_b32 a64, v48\n\t"
    "v_perm_b32 v48, v206, v8, s52\n\t"
    "v_mfma_f32_16x16x32_bf16 v[176:179], a[152:155], a[48:51], v[176:179]\n\t"
    "ds_read_b128 a[184:187], v41 offset:28672\n\t"
    "v_accvgpr_read_b32 v69, a197\n\t"
    "v_mfma_f32_16x16x32_bf16 v[180:183], a[152:155], a[52:55], v[180:183]\n\t"
    "ds_read_b32 a65, v49\n\t"
    "v_perm_b32 v49, v206, v8, s53\n\t"
    "v_mfma_f32_16x16x32_bf16 v[184:187], a[156:159], a[48:51], v[184:187]\n\t"
    "ds_read_b128 a[188:191], v41 offset:30720\n\t"
    "s_waitcnt lgkmcnt(4)\n\t"
    "v_mfma_f32_16x16x32_bf16 v[188:191], a[156:159], a[52:55], v[188:191]\n\t"
    "ds_read_b32 a66, v50\n\t"
    "v_perm_b32 v50, v206, v8, s54\n\t"
    "v_mfma_f32_16x16x32_bf16 v[128:131], a[160:163], a[56:59], v[128:131]\n\t"
    "ds_read_b128 a[144:147], v24 offset:0\n\t"
    "v_accvgpr_read_b32 v70, a198\n\t"
    "v_mfma_f32_16x16x32_bf16 v[132:135], a[160:163], a[60:63], v[132:135]\n\t"
    "ds_read_b32 a67, v51\n\t"
    "v_perm_b32 v51, v206, v8, s55\n\t"
    "v_mfma_f32_16x16x32_bf16 v[136:139], a[164:167], a[56:59], v[136:139]\n\t"
    "ds_read_b128 a[148:151], v24 offset:2048\n\t"
    "v_accvgpr_read_b32 v71, a199\n\t"
    "v_mfma_f32_16x16x32_bf16 v[140:143], a[164:167], a[60:63], v[140:143]\n\t"
    "ds_read_b32 a68, v52\n\t"
    "v_perm_b32 v52, v207, v8, s52\n\t"
    "v_mfma_f32_16x16x32_bf16 v[144:147], a[168:171], a[56:59], v[144:147]\n\t"
    "ds_read_b128 a[152:155], v24 offset:4096\n\t"
    "s_waitcnt vmcnt(2)\n\t"
    "L_chk1_%=:\n\t"
    "v_cmp_ne_u32 s[96:97], v21, v22 ; which flags have changed since entry\n\t"
    "s_andn2_b32 s29, s43, s96\n\t"
    "s_cbranch_scc1 L_spin1_%= ; not all yet: poll again (bounded)\n\t"
    "L_join1_%=:\n\t"
    "v_mfma_f32_16x16x32_bf16 v[148:151], a[168:171], a[60:63], v[148:151]\n\t"
    "buffer_load_dwordx4 v[212:215], v249, s[92:95], s49 offen sc1 ; packed chunk\n\t"
    "v_accvgpr_read_b32 v72, a200\n\t"
    "v_mfma_f32_16x16x32_bf16 v[152:155], a[172:175], a[56:59], v[152:155]\n\t"
    "ds_read_b128 a[156:159], v24 offset:6144\n\t"
    "s_waitcnt lgkmcnt(7)\n\t"
    "v_mfma_f32_16x16x32_bf16 v[156:159], a[172:175], a[60:63], v[156:159]\n\t"
    "ds_read_b32 a69, v53\n\t"
    "v_perm_b32 v53, v207, v8, s53\n\t"
    "v_mfma_f32_16x16x32_bf16 v[160:163], a[176:179], a[56:59], v[160:163]\n\t"
    "ds_read_b128 a[160:163], v24 offset:8192\n\t"
    "v_accvgpr_read_b32 v73, a201\n\t"
    "v_mfma_f32_16x16x32_bf16 v[164:167], a[176:179], a[60:63], v[164:167]\n\t"
    "buffer_load_dwordx4 v[216:219], v249, s[92:95], s50 offen sc1 ; packed chunk\n\t"
    "ds_read_b32 a70, v54\n\t"
    "v_perm_b32 v54, v207, v8, s54\n\t"
    "v_mfma_f32_16x16x32_bf16 v[168:171], a[180:183], a[56:59], v[168:171]\n\t"
    "ds_read_b128 a[164:167], v24 offset:10240\n\t"
    "ds_read_b32 a71, v55\n\t"
    "v_perm_b32 v55, v207, v8, s55\n\t"
    "v_mfma_f32_16x16x32_bf16 v[172:175], a[180:183], a[60:63], v[172:175]\n\t"
    "ds_read_b32 a72, v48\n\t"
    "v_mfma_f32_16x16x32_bf16 v[176:179], a[184:187], a[56:59], v[176:179]\n\t"
    "ds_read_b128 a[168:171], v24 offset:12288\n\t"
    "v_perm_b32 v48, v208, v8, s52\n\t"
    "v_mfma_f32_16x16x32_bf16 v[180:183], a[184:187], a[60:63], v[180:183]\n\t"
    "buffer_load_dwordx4 v[220:223], v249, s[92:95], s51 offen sc1 ; packed chunk\n\t"
    "v_accvgpr_read_b32 v74, a202\n\t"
    "v_mfma_f32_16x16x32_bf16 v[184:187], a[188:191], a[56:59], v[184:187]\n\t"
    "ds_read_b128 a[172:175], v24 offset:14336\n\t"
    "s_waitcnt lgkmcnt(3)\n\t"
    "v_mfma_f32_16x16x32_bf16 v[188:191], a[188:191], a[60:63], v[188:191]\n\t"
    "ds_read_b32 a73, v49\n\t"
    "v_perm_b32 v49, v208, v8, s53\n\t"
    "v_mfma_f32_16x16x32_bf16 v[128:131], a[144:147], a[64:67], v[128:131]\n\t"
    "ds_read_b128 a[176:179], v25 offset:0\n\t"
    "v_accvgpr_read_b32 v75, a203\n\t"
    "v_mfma_f32_16x16x32_bf16 v[132:135], a[144:147], a[68:71], v[132:135]\n\t"
    "ds_read_b32 a74, v50\n\t"
    "v_perm_b32 v50, v208, v8, s54\n\t"
    "v_mfma_f32_16x16x32_bf16 v[136:139], a[148:151], a[64:67], v[136:139]\n\t"
    "ds_read_b128 a[180:183], v25 offset:2048\n\t"
    "v_accvgpr_read_b32 v76, a204\n\t"
    "v_mfma_f32_16x16x32_bf16 v[140:143], a[148:151], a[68:71], v[140:143]\n\t"
    "ds_read_b32 a75, v51\n\t"
    "v_perm_b32 v51, v208, v8, s55\n\t"
    "v_mfma_f32_16x16x32_bf16 v[144:147], a[152:155], a[64:67], v[144:147]\n\t"
    "ds_read_b128 a[184:187], v25 offset:4096\n\t"
    "v_accvgpr_read_b32 v77, a205\n\t"
    "v_mfma_f32_16x16x32_bf16 v[148:151], a[152:155], a[68:71], v[148:151]\n\t"
    "ds_read_b32 a76, v52\n\t"
    "v_perm_b32 v52, v209, v8, s52\n\t"
    "v_mfma_f32_16x16x32_bf16 v[152:155], a[156:159], a[64:67], v[152:155]\n\t"
    "ds_read_b128 a[188:191], v25 offset:6144\n\t"
    "s_waitcnt lgkmcnt(8)\n\t"
    "v_mfma_f32_16x16x32_bf16 v[156:159], a[156:159], a[68:71], v[156:159]\n\t"
    "ds_read_b32 a77, v53\n\t"
    "v_perm_b32 v53, v209, v8, s53\n\t"
    "v_mfma_f32_16x16x32_bf16 v[160:163], a[160:163], a[64:67], v[160:163]\n\t"
    "ds_read_b128 a[144:147], v25 offset:8192\n\t"
    "v_and_b32 v9, 0xffff0000, v35\n\t"
    "v_lshlrev_b32 v35, 16, v35\n\t"
    "v_mfma_f32_16x16x32_bf16 v[164:167], a[160:163], a[68:71], v[164:167]\n\t"
    "ds_read_b32 a78, v54\n\t"
    "v_perm_b32 v54, v209, v8, s54\n\t"
    "v_mfma_f32_16x16x32_bf16 v[168:171], a[164:167], a[64:67], v[168:171]\n\t"
    "ds_read_b128 a[148:151], v25 offset:10240\n\t"
    "ds_read_b32 a79, v55\n\t"
    "v_perm_b32 v55, v209, v8, s55\n\t"
    "v_mfma_f32_16x16x32_bf16 v[172:175], a[164:167], a[68:71], v[172:175]\n\t"
    "ds_read_b32 a80, v48\n\t"
    "v_perm_b32 v48, v210, v8, s52\n\t"
    "v_mfma_f32_16x16x32_bf16 v[176:179], a[168:171], a[64:67], v[176:179]\n\t"
    "ds_read_b128 a[152:155], v25 offset:12288\n\t"
    "v_accvgpr_read_b32 v78, a206\n\t"
    "v_mfma_f32_16x16x32_bf16 v[180:183], a[168:171], a[68:71], v[180:183]\n\t"
    "ds_read_b32 a81, v49\n\t"
    "v_perm_b32 v49, v210, v8, s53\n\t"
    "v_mfma_f32_16x16x32_bf16 v[184:187], a[172:175], a[64:67], v[184:187]\n\t"
    "ds_read_b128 a[156:159], v25 offset:14336\n\t"
    "s_waitcnt vmcnt(12) lgkmcnt(4)\n\t"
    "v_mfma_f32_16x16x32_bf16 v[188:191], a[172:175], a[68:71], v[188:191]\n\t"
    "ds_read_b32 a82, v50\n\t"
    "s_barrier\n\t"
    "v_mfma_f32_16x16x32_bf16 v[128:131], a[176:179], a[72:75], v[128:131]\n\t"
    "ds_read_b128 a[160:163], v24 offset:16384\n\t"
    "v_perm_b32 v50, v210, v8, s54\n\t"
    "v_mfma_f32_16x16x32_bf16 v[132:135], a[176:179], a[76:79], v[132:135]\n\t"
    "ds_read_b32 a83, v51\n\t"
    "s_add_u32 s8, s42, s84\n\t"
    "s_addc_u32 s9, s85, 0\n\t"
    "v_mfma_f32_16x16x32_bf16 v[136:139], a[180:183], a[72:75], v[136:139]\n\t"
    "ds_read_b128 a[164:167], v24 offset:18432\n\t"
    "s_add_u32 m0, s26, 131072\n\t"
    "v_mfma_f32_16x16x32_bf16 v[140:143], a[180:183], a[76:79], v[140:143]\n\t"
    "buffer_load_dwordx4 v2, s[8:11], 0 offen offset:0 lds\n\t"
    "v_perm_b32 v51, v210, v8, s55\n\t"
    "v_mfma_f32_16x16x32_bf16 v[144:147], a[184:187], a[72:75], v[144:147]\n\t"
    "ds_read_b128 a[168:171], v24 offset:20480\n\t"
    "v_accvgpr_read_b32 v79, a207\n\t"
    "v_mfma_f32_16x16x32_bf16 v[148:151], a[184:187], a[76:79], v[148:151]\n\t"
    "ds_read_b32 a84, v52\n\t"
    "v_perm_b32 v52, v211, v8, s52\n\t"
    "v_mfma_f32_16x16x32_bf16 v[152:155], a[188:191], a[72:75], v[152:155]\n\t"
    "ds_read_b128 a[172:175], v24 offset:22528\n\t"
    "s_waitcnt lgkmcnt(7)\n\t"
    "v_mfma_f32_16x16x32_bf16 v[156:159], a[188:191], a[76:79], v[156:159]\n\t"
    "ds_read_b32 a85, v53\n\t"
    "v_perm_b32 v53, v211, v8, s53\n\t"
    "v_mfma_f32_16x16x32_bf16 v[160:163], a[144:147], a[72:75], v[160:163]\n\t"
    "ds_read_b128 a[176:179], v24 offset:24576\n\t"
    "buffer_load_dwordx4 v17, s[8:11], 0 offen offset:1024 lds\n\t"
    "v_accvgpr_read_b32 v80, a208\n\t"
    "v_mfma_f32_16x16x32_bf16 v[164:167], a[144:147], a[76:79], v[164:167]\n\t"
    "ds_read_b32 a86, v54\n\t"
    "v_perm_b32 v54, v211, v8, s54\n\t"
    "v_mfma_f32_16x16x32_bf16 v[168:171], a[148:151], a[72:75], v[168:171]\n\t"
    "ds_read_b128 a[180:183], v24 offset:26624\n\t"
    "ds_read_b32 a87, v55\n\t"
    "v_perm_b32 v55, v211, v8, s55\n\t"
    "v_mfma_f32_16x16x32_bf16 v[172:175], a[148:151], a[76:79], v[172:175]\n\t"
    "ds_read_b32 a88, v48\n\t"
    "s_waitcnt vmcnt(4)\n\t"
    "v_mfma_f32_16x16x32_bf16 v[176:179], a[152:155], a[72:75], v[176:179]\n\t"
    "ds_read_b128 a[184:187], v24 offset:28672\n\t"
    "v_perm_b32 v48, v212, v8, s52\n\t"
    "v_mfma_f32_16x16x32_bf16 v[180:183], a[152:155], a[76:79], v[180:183]\n\t"
    "buffer_load_dwordx4 v18, s[8:11], 0 offen offset:2048 lds\n\t"
    "v_accvgpr_read_b32 v81, a209\n\t"
    "v_mfma_f32_16x16x32_bf16 v[184:187], a[156:159], a[72:75], v[184:187]\n\t"
    "ds_read_b128 a[188:191], v24 offset:30720\n\t"
    "s_waitcnt lgkmcnt(3)\n\t"
    "v_mfma_f32_16x16x32_bf16 v[188:191], a[156:159], a[76:79], v[188:191]\n\t"
    "ds_read_b32 a89, v49\n\t"
    "v_perm_b32 v49, v212, v8, s53\n\t"
    "v_mfma_f32_16x16x32_bf16 v[128:131], a[160:163], a[80:83], v[128:131]\n\t"
    "ds_read_b128 a[144:147], v25 offset:16384\n\t"
    "v_accvgpr_read_b32 v82, a210\n\t"
    "v_mfma_f32_16x16x32_bf16 v[132:135], a[160:163], a[84:87], v[132:135]\n\t"
    "ds_read_b32 a90, v50\n\t"
    "v_perm_b32 v50, v212, v8, s54\n\t"
    "v_mfma_f32_16x16x32_bf16 v[136:139], a[164:167], a[80:83], v[136:139]\n\t"
    "ds_read_b128 a[148:151], v25 offset:18432\n\t"
    "buffer_load_dwordx4 v19, s[8:11], 0 offen offset:3072 lds\n\t"
    "v_accvgpr_read_b32 v83, a211\n\t"
    "v_mfma_f32_16x16x32_bf16 v[140:143], a[164:167], a[84:87], v[140:143]\n\t"
    "ds_read_b32 a91, v51\n\t"
    "v_perm_b32 v51, v212, v8, s55\n\t"
    "v_mfma_f32_16x16x32_bf16 v[144:147], a[168:171], a[80:83], v[144:147]\n\t"
    "ds_read_b128 a[152:155], v25 offset:20480\n\t"
    "v_accvgpr_read_b32 v84, a212\n\t"
    "v_mfma_f32_16x16x32_bf16 v[148:151], a[168:171], a[84:87], v[148:151]\n\t"
    "ds_read_b32 a92, v52\n\t"
    "v_perm_b32 v52, v213, v8, s52\n\t"
    "v_mfma_f32_16x16x32_bf16 v[152:155], a[172:175], a[80:83], v[152:155]\n\t"
    "ds_read_b128 a[156:159], v25 offset:22528\n\t"
    "s_add_u32 s84, s27, s84\n\t"
    "s_addc_u32 s85, s85, 0\n\t"
    "s_add_u32 s80, 147456, s84\n\t"
    "s_addc_u32 s81, s85, 0\n\t"
    "s_add_u32 m0, s75, 147456\n\t"
    "s_waitcnt lgkmcnt(8)\n\t"
    "v_mfma_f32_16x16x32_bf16 v[156:159], a[172:175], a[84:87], v[156:159]\n\t"
    "ds_read_b32 a93, v53\n\t"
    "v_perm_b32 v53, v213, v8, s53\n\t"
    "v_mfma_f32_16x16x32_bf16 v[160:163], a[176:179], a[80:83], v[160:163]\n\t"
    "ds_read_b128 a[160:163], v25 offset:24576\n\t"
    "v_accvgpr_read_b32 v85, a213\n\t"
    "v_mfma_f32_16x16x32_bf16 v[164:167], a[176:179], a[84:87], v[164:167]\n\t"
    "buffer_load_dwordx4 v224, s[80:83], 0 offen offset:0 lds\n\t"
    "ds_read_b32 a94, v54\n\t"
    "v_perm_b32 v54, v213, v8, s54\n\t"
    "v_mfma_f32_16x16x32_bf16 v[168:171], a[180:183], a[80:83], v[168:171]\n\t"
    "ds_read_b128 a[164:167], v25 offset:26624\n\t"
    "ds_read_b32 a95, v55\n\t"
    "v_perm_b32 v55, v213, v8, s55\n\t"
    "v_mfma_f32_16x16x32_bf16 v[172:175], a[180:183], a[84:87], v[172:175]\n\t"
    "ds_read_b32 a96, v48\n\t"
    "v_perm_b32 v48, v214, v8, s52\n\t"
    "v_mfma_f32_16x16x32_bf16 v[176:179], a[184:187], a[80:83], v[176:179]\n\t"
    "ds_read_b128 a[168:171], v25 offset:28672\n\t"
    "v_accvgpr_read_b32 v86, a214\n\t"
    "v_mfma_f32_16x16x32_bf16 v[180:183], a[184:187], a[84:87], v[180:183]\n\t"
    "ds_read_b32 a97, v49\n\t"
    "v_perm_b32 v49, v214, v8, s53\n\t"
    "v_mfma_f32_16x16x32_bf16 v[184:187], a[188:191], a[80:83], v[184:187]\n\t"
    "ds_read_b128 a[172:175], v25 offset:30720\n\t"
    "s_waitcnt vmcnt(13) lgkmcnt(4)\n\t"
    "v_mfma_f32_16x16x32_bf16 v[188:191], a[188:191], a[84:87], v[188:191]\n\t"
    "L_pg2_%=:\n\t"
    "s_cbranch_execz L_bk2_%=\n\t"
    "buffer_load_dwordx4 v225, s[80:83], 0 offen offset:1024 lds\n\t"
    "s_barrier\n\t"
    "v_mfma_f32_16x16x32_bf16 v[128:131], a[144:147], a[88:91], v[128:131]\n\t"
    "ds_read_b128 a[176:179], v24 offset:32768\n\t"
    "v_accvgpr_read_b32 v87, a215\n\t"
    "v_mfma_f32_16x16x32_bf16 v[132:135], a[144:147], a[92:95], v[132:135]\n\t"
    "ds_read_b32 a98, v50\n\t"
    "v_perm_b32 v50, v214, v8, s54\n\t"
    "v_mfma_f32_16x16x32_bf16 v[136:139], a[148:151], a[88:91], v[136:139]\n\t"
    "ds_read_b128 a[180:183], v24 offset:34816\n\t"
    "v_accvgpr_read_b32 v88, a216\n\t"
    "v_mfma_f32_16x16x32_bf16 v[140:143], a[148:151], a[92:95], v[140:143]\n\t"
    "ds_read_b32 a99, v51\n\t"
    "v_perm_b32 v51, v214, v8, s55\n\t"
    "v_mfma_f32_16x16x32_bf16 v[144:147], a[152:155], a[88:91], v[144:147]\n\t"
    "ds_read_b128 a[184:187], v24 offset:36864\n\t"
    "v_accvgpr_read_b32 v89, a217\n\t"
    "v_mfma_f32_16x16x32_bf16 v[148:151], a[152:155], a[92:95], v[148:151]\n\t"
    "buffer_load_dwordx4 v226, s[80:83], 0 offen offset:2048 lds\n\t"
    "s_add_u32 m0, s75, 65536\n\t"
    "v_mfma_f32_16x16x32_bf16 v[152:155], a[156:159], a[88:91], v[152:155]\n\t"
    "ds_read_b128 a[188:191], v24 offset:38912\n\t"
    "s_waitcnt lgkmcnt(6)\n\t"
    "v_mfma_f32_16x16x32_bf16 v[156:159], a[156:159], a[92:95], v[156:159]\n\t"
    "ds_read_b32 a100, v52\n\t"
    "v_perm_b32 v52, v215, v8, s52\n\t"
    "v_mfma_f32_16x16x32_bf16 v[160:163], a[160:163], a[88:91], v[160:163]\n\t"
    "ds_read_b128 a[144:147], v24 offset:40960\n\t"
    "ds_read_b32 a101, v53\n\t"
    "v_perm_b32 v53, v215, v8, s53\n\t"
    "v_mfma_f32_16x16x32_bf16 v[164:167], a[160:163], a[92:95], v[164:167]\n\t"
    "ds_read_b32 a102, v54\n\t"
    "v_perm_b32 v54, v215, v8, s54\n\t"
    "v_mfma_f32_16x16x32_bf16 v[168:171], a[164:167], a[88:91], v[168:171]\n\t"
    "ds_read_b128 a[148:151], v24 offset:43008\n\t"
    "ds_read_b32 a103, v55\n\t"
    "v_perm_b32 v55, v215, v8, s55\n\t"
    "v_mfma_f32_16x16x32_bf16 v[172:175], a[164:167], a[92:95], v[172:175]\n\t"
    "buffer_load_dwordx4 v227, s[80:83], 0 offen offset:0 lds\n\t"
    "s_waitcnt vmcnt(9)\n\t"
    "v_mfma_f32_16x16x32_bf16 v[176:179], a[168:171], a[88:91], v[176:179]\n\t"
    "ds_read_b128 a[152:155], v24 offset:45056\n\t"
    "v_accvgpr_read_b32 v90, a218\n\t"
    "v_mfma_f32_16x16x32_bf16 v[180:183], a[168:171], a[92:95], v[180:183]\n\t"
    "ds_read_b32 a104, v48\n\t"
    "v_perm_b32 v48, v216, v8, s52\n\t"
    "v_mfma_f32_16x16x32_bf16 v[184:187], a[172:175], a[88:91], v[184:187]\n\t"
    "ds_read_b128 a[156:159], v24 offset:47104\n\t"
    "s_waitcnt lgkmcnt(3)\n\t"
    "v_mfma_f32_16x16x32_bf16 v[188:191], a[172:175], a[92:95], v[188:191]\n\t"
    "ds_read_b32 a105, v49\n\t"
    "v_perm_b32 v49, v216, v8, s53\n\t"
    "v_mfma_f32_16x16x32_bf16 v[128:131], a[176:179], a[96:99], v[128:131]\n\t"
    "ds_read_b128 a[160:163], v25 offset:32768\n\t"
    "v_accvgpr_read_b32 v91, a219\n\t"
    "v_mfma_f32_16x16x32_bf16 v[132:135], a[176:179], a[100:103], v[132:135]\n\t"
    "buffer_load_dwordx4 v228, s[80:83], 0 offen offset:1024 lds\n\t"
    "v_accvgpr_read_b32 v92, a220\n\t"
    "v_mfma_f32_16x16x32_bf16 v[136:139], a[180:183], a[96:99], v[136:139]\n\t"
    "ds_read_b128 a[164:167], v25 offset:34816\n\t"
    "v_accvgpr_read_b32 v93, a221\n\t"
    "v_mfma_f32_16x16x32_bf16 v[140:143], a[180:183], a[100:103], v[140:143]\n\t"
    "ds_read_b32 a106, v50\n\t"
    "v_perm_b32 v50, v216, v8, s54\n\t"
    "v_mfma_f32_16x16x32_bf16 v[144:147], a[184:187], a[96:99], v[144:147]\n\t"
    "ds_read_b128 a[168:171], v25 offset:36864\n\t"
    "v_accvgpr_read_b32 v94, a222\n\t"
    "v_mfma_f32_16x16x32_bf16 v[148:151], a[184:187], a[100:103], v[148:151]\n\t"
    "buffer_load_dword v62, v3, s[76:79], 0 offen\n\t"
    "v_accvgpr_read_b32 v95, a223\n\t"
    "v_mfma_f32_16x16x32_bf16 v[152:155], a[188:191], a[96:99], v[152:155]\n\t"
    "ds_read_b128 a[172:175], v25 offset:38912\n\t"
    "ds_read_b32 a107, v51\n\t"
    "s_waitcnt lgkmcnt(7)\n\t"
    "v_mfma_f32_16x16x32_bf16 v[156:159], a[188:191], a[100:103], v[156:159]\n\t"
    "buffer_load_dwordx4 v229, s[80:83], 0 offen offset:2048 lds\n\t"
    "ds_read_b32 a108, v52\n\t"
    "s_add_u32 m0, s75, 81920\n\t"
    "v_mfma_f32_16x16x32_bf16 v[160:163], a[144:147], a[96:99], v[160:163]\n\t"
    "ds_read_b128 a[176:179], v25 offset:40960\n\t"
    "ds_read_b32 a109, v53\n\t"
    "v_perm_b32 v51, v216, v8, s55\n\t"
    "v_mfma_f32_16x16x32_bf16 v[164:167], a[144:147], a[100:103], v[164:167]\n\t"
    "ds_read_b32 a110, v54\n\t"
    "v_perm_b32 v52, v217, v8, s52\n\t"
    "v_mfma_f32_16x16x32_bf16 v[168:171], a[148:151], a[96:99], v[168:171]\n\t"
    "ds_read_b128 a[180:183], v25 offset:43008\n\t"
    "ds_read_b32 a111, v55\n\t"
    "v_perm_b32 v53, v217, v8, s53\n\t"
    "v_mfma_f32_16x16x32_bf16 v[172:175], a[148:151], a[100:103], v[172:175]\n\t"
    "ds_read_b32 a112, v48\n\t"
    "v_perm_b32 v54, v217, v8, s54\n\t"
    "v_mfma_f32_16x16x32_bf16 v[176:179], a[152:155], a[96:99], v[176:179]\n\t"
    "ds_read_b128 a[184:187], v25 offset:45056\n\t"
    "v_perm_b32 v55, v217, v8, s55\n\t"
    "v_mfma_f32_16x16x32_bf16 v[180:183], a[152:155], a[100:103], v[180:183]\n\t"
    "ds_read_b32 a113, v49\n\t"
    "v_perm_b32 v48, v218, v8, s52\n\t"
    "v_mfma_f32_16x16x32_bf16 v[184:187], a[156:159], a[96:99], v[184:187]\n\t"
    "ds_read_b128 a[188:191], v25 offset:47104\n\t"
    "s_waitcnt vmcnt(14) lgkmcnt(4)\n\t"
    "v_mfma_f32_16x16x32_bf16 v[188:191], a[156:159], a[100:103], v[188:191]\n\t"
    "ds_read_b32 a114, v50\n\t"
    "s_barrier\n\t"
    "v_mfma_f32_16x16x32_bf16 v[128:131], a[160:163], a[104:107], v[128:131]\n\t"
    "ds_read_b128 a[144:147], v24 offset:49152\n\t"
    "v_perm_b32 v49, v218, v8, s53\n\t"
    "v_mfma_f32_16x16x32_bf16 v[132:135], a[160:163], a[108:111], v[132:135]\n\t"
    "buffer_load_dwordx4 v230, s[80:83], 0 offen offset:0 lds\n\t"
    "v_perm_b32 v50, v218, v8, s54\n\t"
    "v_mfma_f32_16x16x32_bf16 v[136:139], a[164:167], a[104:107], v[136:139]\n\t"
    "ds_read_b128 a[148:151], v24 offset:51200\n\t"
    "v_accvgpr_read_b32 v96, a224\n\t"
    "v_mfma_f32_16x16x32_bf16 v[140:143], a[164:167], a[108:111], v[140:143]\n\t"
    "ds_read_b32 a115, v51\n\t"
    "v_perm_b32 v51, v218, v8, s55\n\t"
    "v_mfma_f32_16x16x32_bf16 v[144:147], a[168:171], a[104:107], v[144:147]\n\t"
    "ds_read_b128 a[152:155], v24 offset:53248\n\t"
    "v_accvgpr_read_b32 v97, a225\n\t"
    "v_mfma_f32_16x16x32_bf16 v[148:151], a[168:171], a[108:111], v[148:151]\n\t"
    "ds_read_b32 a116, v52\n\t"
    "v_perm_b32 v52, v219, v8, s52\n\t"
    "v_mfma_f32_16x16x32_bf16 v[152:155], a[172:175], a[104:107], v[152:155]\n\t"
    "ds_read_b128 a[156:159], v24 offset:55296\n\t"
    "s_waitcnt lgkmcnt(7)\n\t"
    "v_mfma_f32_16x16x32_bf16 v[156:159], a[172:175], a[108:111], v[156:159]\n\t"
    "buffer_load_dwordx4 v231, s[80:83], 0 offen offset:1024 lds\n\t"
    "v_accvgpr_read_b32 v98, a226\n\t"
    "v_mfma_f32_16x16x32_bf16 v[160:163], a[176:179], a[104:107], v[160:163]\n\t"
    "ds_read_b128 a[160:163], v24 offset:57344\n\t"
    "ds_read_b32 a117, v53\n\t"
    "v_perm_b32 v53, v219, v8, s53\n\t"
    "v_mfma_f32_16x16x32_bf16 v[164:167], a[176:179], a[108:111], v[164:167]\n\t"
    "ds_read_b32 a118, v54\n\t"
    "v_perm_b32 v54, v219, v8, s54\n\t"
    "v_mfma_f32_16x16x32_bf16 v[168:171], a[180:183], a[104:107], v[168:171]\n\t"
    "ds_read_b128 a[164:167], v24 offset:59392\n\t"
    "ds_read_b32 a119, v55\n\t"
    "v_perm_b32 v55, v219, v8, s55\n\t"
    "v_mfma_f32_16x16x32_bf16 v[172:175], a[180:183], a[108:111], v[172:175]\n\t"
    "ds_read_b32 a120, v48\n\t"
    "s_waitcnt vmcnt(13)\n\t"
    "v_mfma_f32_16x16x32_bf16 v[176:179], a[184:187], a[104:107], v[176:179]\n\t"
    "ds_read_b128 a[168:171], v24 offset:61440\n\t"
    "v_perm_b32 v48, v220, v8, s52\n\t"
    "v_mfma_f32_16x16x32_bf16 v[180:183], a[184:187], a[108:111], v[180:183]\n\t"
    "buffer_load_dwordx4 v232, s[80:83], 0 offen offset:2048 lds\n\t"
    "v_accvgpr_read_b32 v99, a227\n\t"
    "v_mfma_f32_16x16x32_bf16 v[184:187], a[188:191], a[104:107], v[184:187]\n\t"
    "ds_read_b128 a[172:175], v24 offset:63488\n\t"
    "s_waitcnt lgkmcnt(3)\n\t"
    "v_mfma_f32_16x16x32_bf16 v[188:191], a[188:191], a[108:111], v[188:191]\n\t"
    "ds_read_b32 a121, v49\n\t"
    "v_perm_b32 v49, v220, v8, s53\n\t"
    "v_mfma_f32_16x16x32_bf16 v[128:131], a[144:147], a[112:115], v[128:131]\n\t"
    "ds_read_b128 a[176:179], v25 offset:49152\n\t"
    "v_accvgpr_read_b32 v100, a228\n\t"
    "v_mfma_f32_16x16x32_bf16 v[132:135], a[144:147], a[116:119], v[132:135]\n\t"
    "ds_read_b32 a122, v50\n\t"
    "s_add_u32 s80, 0, s84\n\t"
    "s_addc_u32 s81, s85, 0\n\t"
    "s_add_u32 m0, s75, 98304\n\t"
    "v_mfma_f32_16x16x32_bf16 v[136:139], a[148:151], a[112:115], v[136:139]\n\t"
    "ds_read_b128 a[180:183], v25 offset:51200\n\t"
    "v_perm_b32 v50, v220, v8, s54\n\t"
    "v_mfma_f32_16x16x32_bf16 v[140:143], a[148:151], a[116:119], v[140:143]\n\t"
    "buffer_load_dword v62, v20, s[76:79], 0 offen\n\t"
    "v_accvgpr_read_b32 v101, a229\n\t"
    "v_mfma_f32_16x16x32_bf16 v[144:147], a[152:155], a[112:115], v[144:147]\n\t"
    "ds_read_b128 a[184:187], v25 offset:53248\n\t"
    "v_accvgpr_read_b32 v102, a230\n\t"
    "v_mfma_f32_16x16x32_bf16 v[148:151], a[152:155], a[116:119], v[148:151]\n\t"
    "ds_read_b32 a123, v51\n\t"
    "v_perm_b32 v51, v220, v8, s55\n\t"
    "v_mfma_f32_16x16x32_bf16 v[152:155], a[156:159], a[112:115], v[152:155]\n\t"
    "ds_read_b128 a[188:191], v25 offset:55296\n\t"
    "s_waitcnt lgkmcnt(7)\n\t"
    "v_mfma_f32_16x16x32_bf16 v[156:159], a[156:159], a[116:119], v[156:159]\n\t"
    "ds_read_b32 a124, v52\n\t"
    "v_perm_b32 v52, v221, v8, s52\n\t"
    "v_mfma_f32_16x16x32_bf16 v[160:163], a[160:163], a[112:115], v[160:163]\n\t"
    "ds_read_b128 a[144:147], v25 offset:57344\n\t"
    "ds_read_b32 a125, v53\n\t"
    "v_perm_b32 v53, v221, v8, s53\n\t"
    "v_mfma_f32_16x16x32_bf16 v[164:167], a[160:163], a[116:119], v[164:167]\n\t"
    "ds_read_b32 a126, v54\n\t"
    "v_perm_b32 v54, v221, v8, s54\n\t"
    "v_mfma_f32_16x16x32_bf16 v[168:171], a[164:167], a[112:115], v[168:171]\n\t"
    "ds_read_b128 a[148:151], v25 offset:59392\n\t"
    "ds_read_b32 a127, v55\n\t"
    "v_perm_b32 v55, v221, v8, s55\n\t"
    "v_mfma_f32_16x16x32_bf16 v[172:175], a[164:167], a[116:119], v[172:175]\n\t"
    "ds_read_b32 a128, v48\n\t"
    "v_perm_b32 v48, v222, v8, s52\n\t"
    "v_mfma_f32_16x16x32_bf16 v[176:179], a[168:171], a[112:115], v[176:179]\n\t"
    "ds_read_b128 a[152:155], v25 offset:61440\n\t"
    "v_accvgpr_read_b32 v103, a231\n\t"
    "v_mfma_f32_16x16x32_bf16 v[180:183], a[168:171], a[116:119], v[180:183]\n\t"
    "ds_read_b32 a129, v49\n\t"
    "v_perm_b32 v49, v222, v8, s53\n\t"
    "v_mfma_f32_16x16x32_bf16 v[184:187], a[172:175], a[112:115], v[184:187]\n\t"
    "ds_read_b128 a[156:159], v25 offset:63488\n\t"
    "s_waitcnt vmcnt(11) lgkmcnt(4)\n\t"
    "v_mfma_f32_16x16x32_bf16 v[188:191], a[172:175], a[116:119], v[188:191]\n\t"
    "ds_read_b32 a130, v50\n\t"
    "s_barrier\n\t"
    "v_mfma_f32_16x16x32_bf16 v[128:131], a[176:179], a[120:123], v[128:131]\n\t"
    "ds_read_b128 a[160:163], v40 offset:0\n\t"
    "v_perm_b32 v50, v222, v8, s54\n\t"
    "v_mfma_f32_16x16x32_bf16 v[132:135], a[176:179], a[124:127], v[132:135]\n\t"
    "buffer_load_dwordx4 v226, s[80:83], 0 offen offset:2048 lds\n\t"
    "s_add_u32 m0, s75, 0\n\t"
    "v_mfma_f32_16x16x32_bf16 v[136:139], a[180:183], a[120:123], v[136:139]\n\t"
    "ds_read_b128 a[164:167], v40 offset:2048\n\t"
    "v_accvgpr_read_b32 v104, a232\n\t"
    "v_mfma_f32_16x16x32_bf16 v[140:143], a[180:183], a[124:127], v[140:143]\n\t"
    "ds_read_b32 a131, v51\n\t"
    "v_perm_b32 v51, v222, v8, s55\n\t"
    "v_mfma_f32_16x16x32_bf16 v[144:147], a[184:187], a[120:123], v[144:147]\n\t"
    "ds_read_b128 a[168:171], v40 offset:4096\n\t"
    "v_accvgpr_read_b32 v105, a233\n\t"
    "v_mfma_f32_16x16x32_bf16 v[148:151], a[184:187], a[124:127], v[148:151]\n\t"
    "ds_read_b32 a132, v52\n\t"
    "v_perm_b32 v52, v223, v8, s52\n\t"
    "v_mfma_f32_16x16x32_bf16 v[152:155], a[188:191], a[120:123], v[152:155]\n\t"
    "ds_read_b128 a[172:175], v40 offset:6144\n\t"
    "s_waitcnt lgkmcnt(7)\n\t"
    "v_mfma_f32_16x16x32_bf16 v[156:159], a[188:191], a[124:127], v[156:159]\n\t"
    "ds_read_b32 a133, v53\n\t"
    "v_perm_b32 v53, v223, v8, s53\n\t"
    "v_mfma_f32_16x16x32_bf16 v[160:163], a[144:147], a[120:123], v[160:163]\n\t"
    "ds_read_b128 a[176:179], v40 offset:8192\n\t"
    "v_accvgpr_read_b32 v106, a234\n\t"
    "v_mfma_f32_16x16x32_bf16 v[164:167], a[144:147], a[124:127], v[164:167]\n\t"
    "ds_read_b32 a134, v54\n\t"
    "v_perm_b32 v54, v223, v8, s54\n\t"
    "v_mfma_f32_16x16x32_bf16 v[168:171], a[148:151], a[120:123], v[168:171]\n\t"
    "ds_read_b128 a[180:183], v40 offset:10240\n\t"
    "ds_read_b32 a135, v55\n\t"
    "v_perm_b32 v55, v223, v8, s55\n\t"
    "v_mfma_f32_16x16x32_bf16 v[172:175], a[148:151], a[124:127], v[172:175]\n\t"
    "ds_read_b32 a136, v48\n\t"
    "v_accvgpr_read_b32 v107, a235\n\t"
    "v_mfma_f32_16x16x32_bf16 v[176:179], a[152:155], a[120:123], v[176:179]\n\t"
    "ds_read_b128 a[184:187], v40 offset:12288\n\t"
    "v_accvgpr_read_b32 v108, a236\n\t"
    "v_mfma_f32_16x16x32_bf16 v[180:183], a[152:155], a[124:127], v[180:183]\n\t"
    "ds_read_b32 a137, v49\n\t"
    "v_accvgpr_read_b32 v109, a237\n\t"
    "v_mfma_f32_16x16x32_bf16 v[184:187], a[156:159], a[120:123], v[184:187]\n\t"
    "ds_read_b128 a[188:191], v40 offset:14336\n\t"
    "s_waitcnt lgkmcnt(4)\n\t"
    "v_mfma_f32_16x16x32_bf16 v[188:191], a[156:159], a[124:127], v[188:191]\n\t"
    "ds_read_b32 a138, v50\n\t"
    "v_accvgpr_read_b32 v110, a238\n\t"
    "v_mfma_f32_16x16x32_bf16 v[128:131], a[160:163], a[128:131], v[128:131]\n\t"
    "ds_read_b128 a[144:147], v41 offset:0\n\t"
    "v_accvgpr_read_b32 v111, a239\n\t"
    "v_mfma_f32_16x16x32_bf16 v[132:135], a[160:163], a[132:135], v[132:135]\n\t"
    "ds_read_b32 a139, v51\n\t"
    "v_accvgpr_read_b32 v112, a240\n\t"
    "v_mfma_f32_16x16x32_bf16 v[136:139], a[164:167], a[128:131], v[136:139]\n\t"
    "ds_read_b128 a[148:151], v41 offset:2048\n\t"
    "v_accvgpr_read_b32 v113, a241\n\t"
    "v_mfma_f32_16x16x32_bf16 v[140:143], a[164:167], a[132:135], v[140:143]\n\t"
    "ds_read_b32 a140, v52\n\t"
    "v_accvgpr_read_b32 v114, a242\n\t"
    "v_mfma_f32_16x16x32_bf16 v[144:147], a[168:171], a[128:131], v[144:147]\n\t"
    "ds_read_b128 a[152:155], v41 offset:4096\n\t"
    "v_accvgpr_read_b32 v115, a243\n\t"
    "v_mfma_f32_16x16x32_bf16 v[148:151], a[168:171], a[132:135], v[148:151]\n\t"
    "ds_read_b32 a141, v53\n\t"
    "v_accvgpr_read_b32 v116, a244\n\t"
    "v_mfma_f32_16x16x32_bf16 v[152:155], a[172:175], a[128:131], v[152:155]\n\t"
    "ds_read_b128 a[156:159], v41 offset:6144\n\t"
    "s_waitcnt lgkmcnt(8)\n\t"
    "v_mfma_f32_16x16x32_bf16 v[156:159], a[172:175], a[132:135], v[156:159]\n\t"
    "ds_read_b32 a142, v54\n\t"
    "v_accvgpr_read_b32 v117, a245\n\t"
    "v_mfma_f32_16x16x32_bf16 v[160:163], a[176:179], a[128:131], v[160:163]\n\t"
    "ds_read_b128 a[160:163], v41 offset:8192\n\t"
    "v_accvgpr_read_b32 v118, a246\n\t"
    "v_mfma_f32_16x16x32_bf16 v[164:167], a[176:179], a[132:135], v[164:167]\n\t"
    "ds_read_b32 a143, v55\n\t"
    "v_accvgpr_read_b32 v119, a247\n\t"
    "v_mfma_f32_16x16x32_bf16 v[168:171], a[180:183], a[128:131], v[168:171]\n\t"
    "ds_read_b128 a[164:167], v41 offset:10240\n\t"
    "v_accvgpr_read_b32 v120, a248\n\t"
    "v_mfma_f32_16x16x32_bf16 v[172:175], a[180:183], a[132:135], v[172:175]\n\t"
    "v_accvgpr_read_b32 v121, a249\n\t"
    "v_accvgpr_read_b32 v122, a250\n\t"
    "v_mfma_f32_16x16x32_bf16 v[176:179], a[184:187], a[128:131], v[176:179]\n\t"
    "ds_read_b128 a[168:171], v41 offset:12288\n\t"
    "v_accvgpr_read_b32 v123, a251\n\t"
    "v_mfma_f32_16x16x32_bf16 v[180:183], a[184:187], a[132:135], v[180:183]\n\t"
    "v_accvgpr_read_b32 v124, a252\n\t"
    "v_accvgpr_read_b32 v125, a253\n\t"
    "v_mfma_f32_16x16x32_bf16 v[184:187], a[188:191], a[128:131], v[184:187]\n\t"
    "ds_read_b128 a[172:175], v41 offset:14336\n\t"
    "s_waitcnt vmcnt(9) lgkmcnt(3)\n\t"
    "v_mfma_f32_16x16x32_bf16 v[188:191], a[188:191], a[132:135], v[188:191]\n\t"
    "s_barrier\n\t"
    "v_accvgpr_read_b32 v126, a254\n\t"
    "v_accvgpr_read_b32 v127, a255\n\t"
    "v_mfma_f32_16x16x32_bf16 v[128:131], a[144:147], a[136:139], v[128:131]\n\t"
    "ds_read_b128 a[176:179], v30 offset:16384\n\t"
    "v_mfma_f32_16x16x32_bf16 v[132:135], a[144:147], a[140:143], v[132:135]\n\t"
    "buffer_load_dwordx4 v227, s[80:83], 0 offen offset:0 lds\n\t"
    "v_mfma_f32_16x16x32_bf16 v[136:139], a[148:151], a[136:139], v[136:139]\n\t"
    "ds_read_b128 a[180:183], v30 offset:22528\n\t"
    "v_mfma_f32_16x16x32_bf16 v[140:143], a[148:151], a[140:143], v[140:143]\n\t"
    "v_mfma_f32_16x16x32_bf16 v[144:147], a[152:155], a[136:139], v[144:147]\n\t"
    "ds_read_b128 a[184:187], v31 offset:16384\n\t"
    "v_mfma_f32_16x16x32_bf16 v[148:151], a[152:155], a[140:143], v[148:151]\n\t"
    "v_mfma_f32_16x16x32_bf16 v[152:155], a[156:159], a[136:139], v[152:155]\n\t"
    "ds_read_b128 a[188:191], v31 offset:22528\n\t"
    "s_waitcnt lgkmcnt(4)\n\t"
    "v_mfma_f32_16x16x32_bf16 v[156:159], a[156:159], a[140:143], v[156:159]\n\t"
    "buffer_load_dwordx4 v228, s[80:83], 0 offen offset:1024 lds\n\t"
    "v_mul_f32 v128, v35, v128\n\t"
    "v_mfma_f32_16x16x32_bf16 v[160:163], a[160:163], a[136:139], v[160:163]\n\t"
    "ds_read_b128 a[144:147], v30 offset:17408\n\t"
    "v_mul_f32 v132, v9, v132\n\t"
    "v_mfma_f32_16x16x32_bf16 v[164:167], a[160:163], a[140:143], v[164:167]\n\t"
    "v_cvt_pk_bf16_f32 v128, v128, v132\n\t"
    "v_mul_f32 v129, v35, v129\n\t"
    "v_mfma_f32_16x16x32_bf16 v[168:171], a[164:167], a[136:139], v[168:171]\n\t"
    "ds_read_b128 a[148:151], v30 offset:23552\n\t"
    "v_mul_f32 v133, v9, v133\n\t"
    "v_mfma_f32_16x16x32_bf16 v[172:175], a[164:167], a[140:143], v[172:175]\n\t"
    "buffer_store_dword v128, v4, s[16:19], 0 offen nt\n\t"
    "v_cvt_pk_bf16_f32 v129, v129, v133\n\t"
    "v_mfma_f32_16x16x32_bf16 v[176:179], a[168:171], a[136:139], v[176:179]\n\t"
    "ds_read_b128 a[152:155], v31 offset:17408\n\t"
    "v_mul_f32 v130, v35, v130\n\t"
    "v_mfma_f32_16x16x32_bf16 v[180:183], a[168:171], a[140:143], v[180:183]\n\t"
    "buffer_load_dwordx4 v229, s[80:83], 0 offen offset:2048 lds\n\t"
    "s_add_u32 m0, s75, 12288\n\t"
    "v_mfma_f32_16x16x32_bf16 v[184:187], a[172:175], a[136:139], v[184:187]\n\t"
    "ds_read_b128 a[156:159], v31 offset:23552\n\t"
    "s_waitcnt lgkmcnt(4)\n\t"
    "v_mfma_f32_16x16x32_bf16 v[188:191], a[172:175], a[140:143], v[188:191]\n\t"
    "buffer_store_dword v129, v32, s[16:19], 0 offen nt\n\t"
    "v_mul_f32 v134, v9, v134\n\t"
    "v_mfma_f32_16x16x32_bf16 v[192:195], a[176:179], a[0:3], 0\n\t"
    "ds_read_b128 a[160:163], v30 offset:18432\n\t"
    "v_cvt_pk_bf16_f32 v130, v130, v134\n\t"
    "v_mfma_f32_16x16x32_bf16 v[196:199], a[176:179], a[4:7], 0\n\t"
    "buffer_store_dword v130, v33, s[16:19], 0 offen nt\n\t"
    "v_mul_f32 v131, v35, v131\n\t"
    "v_mfma_f32_16x16x32_bf16 v[200:203], a[180:183], a[0:3], 0\n\t"
    "ds_read_b128 a[164:167], v30 offset:24576\n\t"
    "v_mul_f32 v135, v9, v135\n\t"
    "v_mfma_f32_16x16x32_bf16 v[204:207], a[180:183], a[4:7], 0\n\t"
    "buffer_load_dwordx4 v230, s[80:83], 0 offen offset:0 lds\n\t"
    "v_cvt_pk_bf16_f32 v131, v131, v135\n\t"
    "v_mfma_f32_16x16x32_bf16 v[192:195], a[184:187], a[8:11], v[192:195]\n\t"
    "ds_read_b128 a[168:171], v31 offset:18432\n\t"
    "v_mul_f32 v136, v35, v136\n\t"
    "v_mfma_f32_16x16x32_bf16 v[196:199], a[184:187], a[12:15], v[196:199]\n\t"
    "v_mul_f32 v140, v9, v140\n\t"
    "v_cvt_pk_bf16_f32 v136, v136, v140\n\t"
    "v_mfma_f32_16x16x32_bf16 v[200:203], a[188:191], a[8:11], v[200:203]\n\t"
    "ds_read_b128 a[172:175], v31 offset:24576\n\t"
    "s_waitcnt vmcnt(9) lgkmcnt(4)\n\t"
    "v_mfma_f32_16x16x32_bf16 v[204:207], a[188:191], a[12:15], v[204:207]\n\t"
    "buffer_store_dword v131, v34, s[16:19], 0 offen nt\n\t"
    "s_barrier\n\t"
    "v_mfma_f32_16x16x32_bf16 v[192:195], a[144:147], a[16:19], v[192:195]\n\t"
    "ds_read_b128 a[176:179], v28 offset:0\n\t"
    "v_mul_f32 v137, v35, v137\n\t"
    "v_mfma_f32_16x16x32_bf16 v[196:199], a[144:147], a[20:23], v[196:199]\n\t"
    "buffer_load_dwordx4 v231, s[80:83], 0 offen offset:1024 lds\n\t"
    "v_mul_f32 v141, v9, v141\n\t"
    "v_mfma_f32_16x16x32_bf16 v[200:203], a[148:151], a[16:19], v[200:203]\n\t"
    "ds_read_b128 a[180:183], v28 offset:6144\n\t"
    "v_cvt_pk_bf16_f32 v137, v137, v141\n\t"
    "v_mfma_f32_16x16x32_bf16 v[204:207], a[148:151], a[20:23], v[204:207]\n\t"
    "s_add_u32 s16, 147456, s16\n\t"
    "s_addc_u32 s17, s17, 0\n\t"
    "v_mfma_f32_16x16x32_bf16 v[192:195], a[152:155], a[24:27], v[192:195]\n\t"
    "ds_read_b128 a[184:187], v29 offset:0\n\t"
    "v_mfma_f32_16x16x32_bf16 v[196:199], a[152:155], a[28:31], v[196:199]\n\t"
    "buffer_store_dword v136, v4, s[16:19], 0 offen nt\n\t"
    "v_mul_f32 v138, v35, v138\n\t"
    "v_mfma_f32_16x16x32_bf16 v[200:203], a[156:159], a[24:27], v[200:203]\n\t"
    "ds_read_b128 a[188:191], v29 offset:6144\n\t"
    "s_waitcnt lgkmcnt(4)\n\t"
    "v_mfma_f32_16x16x32_bf16 v[204:207], a[156:159], a[28:31], v[204:207]\n\t"
    "buffer_load_dwordx4 v232, s[80:83], 0 offen offset:2048 lds\n\t"
    "v_mul_f32 v142, v9, v142\n\t"
    "v_mfma_f32_16x16x32_bf16 v[192:195], a[160:163], a[32:35], v[192:195]\n\t"
    "ds_read_b128 a[144:147], v28 offset:1024\n\t"
    "v_cvt_pk_bf16_f32 v138, v138, v142\n\t"
    "v_mfma_f32_16x16x32_bf16 v[196:199], a[160:163], a[36:39], v[196:199]\n\t"
    "s_add_u32 s80, 36864, s84\n\t"
    "s_addc_u32 s81, s85, 0\n\t"
    "s_add_u32 m0, s75, 24576\n\t"
    "v_mfma_f32_16x16x32_bf16 v[200:203], a[164:167], a[32:35], v[200:203]\n\t"
    "ds_read_b128 a[148:151], v28 offset:7168\n\t"
    "v_mul_f32 v139, v35, v139\n\t"
    "v_mfma_f32_16x16x32_bf16 v[204:207], a[164:167], a[36:39], v[204:207]\n\t"
    "buffer_store_dword v137, v32, s[16:19], 0 offen nt\n\t"
    "v_mul_f32 v143, v9, v143\n\t"
    "v_mfma_f32_16x16x32_bf16 v[192:195], a[168:171], a[40:43], v[192:195]\n\t"
    "ds_read_b128 a[152:155], v29 offset:1024\n\t"
    "v_cvt_pk_bf16_f32 v139, v139, v143\n\t"
    "v_mfma_f32_16x16x32_bf16 v[196:199], a[168:171], a[44:47], v[196:199]\n\t"
    "buffer_load_dwordx4 v226, s[80:83], 0 offen offset:2048 lds\n\t"
    "s_add_u32 m0, s75, 36864\n\t"
    "v_mfma_f32_16x16x32_bf16 v[200:203], a[172:175], a[40:43], v[200:203]\n\t"
    "ds_read_b128 a[156:159], v29 offset:7168\n\t"
    "s_waitcnt lgkmcnt(4)\n\t"
    "v_mfma_f32_16x16x32_bf16 v[204:207], a[172:175], a[44:47], v[204:207]\n\t"
    "buffer_store_dword v138, v33, s[16:19], 0 offen nt\n\t"
    "v_mul_f32 v144, v35, v144\n\t"
    "v_mfma_f32_16x16x32_bf16 v[192:195], a[176:179], a[48:51], v[192:195]\n\t"
    "ds_read_b128 a[160:163], v28 offset:2048\n\t"
    "v_mul_f32 v148, v9, v148\n\t"
    "v_mfma_f32_16x16x32_bf16 v[196:199], a[176:179], a[52:55], v[196:199]\n\t"
    "v_cvt_pk_bf16_f32 v144, v144, v148\n\t"
    "v_mfma_f32_16x16x32_bf16 v[200:203], a[180:183], a[48:51], v[200:203]\n\t"
    "ds_read_b128 a[164:167], v28 offset:8192\n\t"
    "v_mfma_f32_16x16x32_bf16 v[204:207], a[180:183], a[52:55], v[204:207]\n\t"
    "buffer_load_dwordx4 v227, s[80:83], 0 offen offset:0 lds\n\t"
    "v_mul_f32 v145, v35, v145\n\t"
    "v_mfma_f32_16x16x32_bf16 v[192:195], a[184:187], a[56:59], v[192:195]\n\t"
    "ds_read_b128 a[168:171], v29 offset:2048\n\t"
    "v_mul_f32 v149, v9, v149\n\t"
    "v_mfma_f32_16x16x32_bf16 v[196:199], a[184:187], a[60:63], v[196:199]\n\t"
    "buffer_store_dword v139, v34, s[16:19], 0 offen nt\n\t"
    "v_cvt_pk_bf16_f32 v145, v145, v149\n\t"
    "v_mfma_f32_16x16x32_bf16 v[200:203], a[188:191], a[56:59], v[200:203]\n\t"
    "ds_read_b128 a[172:175], v29 offset:8192\n\t"
    "s_waitcnt lgkmcnt(4)\n\t"
    "v_mfma_f32_16x16x32_bf16 v[204:207], a[188:191], a[60:63], v[204:207]\n\t"
    "s_add_u32 s16, 147456, s16\n\t"
    "s_addc_u32 s17, s17, 0\n\t"
    "v_mfma_f32_16x16x32_bf16 v[192:195], a[144:147], a[64:67], v[192:195]\n\t"
    "ds_read_b128 a[176:179], v28 offset:16384\n\t"
    "v_mul_f32 v146, v35, v146\n\t"
    "v_mfma_f32_16x16x32_bf16 v[196:199], a[144:147], a[68:71], v[196:199]\n\t"
    "buffer_store_dword v144, v4, s[16:19], 0 offen nt\n\t"
    "v_mul_f32 v150, v9, v150\n\t"
    "v_mfma_f32_16x16x32_bf16 v[200:203], a[148:151], a[64:67], v[200:203]\n\t"
    "ds_read_b128 a[180:183], v28 offset:22528\n\t"
    "v_cvt_pk_bf16_f32 v146, v146, v150\n\t"
    "v_mfma_f32_16x16x32_bf16 v[204:207], a[148:151], a[68:71], v[204:207]\n\t"
    "buffer_load_dwordx4 v228, s[80:83], 0 offen offset:1024 lds\n\t"
    "v_mfma_f32_16x16x32_bf16 v[192:195], a[152:155], a[72:75], v[192:195]\n\t"
    "ds_read_b128 a[184:187], v29 offset:16384\n\t"
    "v_mfma_f32_16x16x32_bf16 v[196:199], a[152:155], a[76:79], v[196:199]\n\t"
    "buffer_store_dword v145, v32, s[16:19], 0 offen nt\n\t"
    "v_mul_f32 v147, v35, v147\n\t"
    "v_mfma_f32_16x16x32_bf16 v[200:203], a[156:159], a[72:75], v[200:203]\n\t"
    "ds_read_b128 a[188:191], v29 offset:22528\n\t"
    "s_waitcnt lgkmcnt(4)\n\t"
    "v_mfma_f32_16x16x32_bf16 v[204:207], a[156:159], a[76:79], v[204:207]\n\t"
    "v_mul_f32 v151, v9, v151\n\t"
    "v_cvt_pk_bf16_f32 v147, v147, v151\n\t"
    "v_mfma_f32_16x16x32_bf16 v[192:195], a[160:163], a[80:83], v[192:195]\n\t"
    "ds_read_b128 a[144:147], v28 offset:17408\n\t"
    "v_mfma_f32_16x16x32_bf16 v[196:199], a[160:163], a[84:87], v[196:199]\n\t"
    "buffer_store_dword v146, v33, s[16:19], 0 offen nt\n\t"
    "v_mfma_f32_16x16x32_bf16 v[200:203], a[164:167], a[80:83], v[200:203]\n\t"
    "ds_read_b128 a[148:151], v28 offset:23552\n\t"
    "v_mul_f32 v152, v35, v152\n\t"
    "v_mfma_f32_16x16x32_bf16 v[204:207], a[164:167], a[84:87], v[204:207]\n\t"
    "buffer_load_dwordx4 v229, s[80:83], 0 offen offset:2048 lds\n\t"
    "s_add_u32 m0, s75, 49152\n\t"
    "v_mfma_f32_16x16x32_bf16 v[192:195], a[168:171], a[88:91], v[192:195]\n\t"
    "ds_read_b128 a[152:155], v29 offset:17408\n\t"
    "v_mul_f32 v156, v9, v156\n\t"
    "v_mfma_f32_16x16x32_bf16 v[196:199], a[168:171], a[92:95], v[196:199]\n\t"
    "buffer_store_dword v147, v34, s[16:19], 0 offen nt\n\t"
    "v_cvt_pk_bf16_f32 v152, v152, v156\n\t"
    "v_mfma_f32_16x16x32_bf16 v[200:203], a[172:175], a[88:91], v[200:203]\n\t"
    "ds_read_b128 a[156:159], v29 offset:23552\n\t"
    "s_waitcnt lgkmcnt(4)\n\t"
    "v_mfma_f32_16x16x32_bf16 v[204:207], a[172:175], a[92:95], v[204:207]\n\t"
    "s_add_u32 s16, 147456, s16\n\t"
    "s_addc_u32 s17, s17, 0\n\t"
    "v_mul_f32 v153, v35, v153\n\t"
    "v_mfma_f32_16x16x32_bf16 v[192:195], a[176:179], a[96:99], v[192:195]\n\t"
    "ds_read_b128 a[160:163], v28 offset:18432\n\t"
    "v_mul_f32 v157, v9, v157\n\t"
    "v_mfma_f32_16x16x32_bf16 v[196:199], a[176:179], a[100:103], v[196:199]\n\t"
    "v_cvt_pk_bf16_f32 v153, v153, v157\n\t"
    "v_mfma_f32_16x16x32_bf16 v[200:203], a[180:183], a[96:99], v[200:203]\n\t"
    "ds_read_b128 a[164:167], v28 offset:24576\n\t"
    "v_mfma_f32_16x16x32_bf16 v[204:207], a[180:183], a[100:103], v[204:207]\n\t"
    "buffer_load_dwordx4 v230, s[80:83], 0 offen offset:0 lds\n\t"
    "v_mfma_f32_16x16x32_bf16 v[192:195], a[184:187], a[104:107], v[192:195]\n\t"
    "ds_read_b128 a[168:171], v29 offset:18432\n\t"
    "v_mul_f32 v154, v35, v154\n\t"
    "v_mfma_f32_16x16x32_bf16 v[196:199], a[184:187], a[108:111], v[196:199]\n\t"
    "buffer_store_dword v152, v4, s[16:19], 0 offen nt\n\t"
    "v_mul_f32 v158, v9, v158\n\t"
    "v_mfma_f32_16x16x32_bf16 v[200:203], a[188:191], a[104:107], v[200:203]\n\t"
    "ds_read_b128 a[172:175], v29 offset:24576\n\t"
    "s_waitcnt vmcnt(13) lgkmcnt(4)\n\t"
    "v_mfma_f32_16x16x32_bf16 v[204:207], a[188:191], a[108:111], v[204:207]\n\t"
    "buffer_store_dword v153, v32, s[16:19], 0 offen nt\n\t"
    "s_barrier\n\t"
    "v_mfma_f32_16x16x32_bf16 v[192:195], a[144:147], a[112:115], v[192:195]\n\t"
    "ds_read_b128 a[176:179], v28 offset:34816\n\t"
    "v_cvt_pk_bf16_f32 v154, v154, v158\n\t"
    "v_mfma_f32_16x16x32_bf16 v[196:199], a[144:147], a[116:119], v[196:199]\n\t"
    "v_mul_f32 v155, v35, v155\n\t"
    "v_mul_f32 v159, v9, v159\n\t"
    "v_mfma_f32_16x16x32_bf16 v[200:203], a[148:151], a[112:115], v[200:203]\n\t"
    "ds_read_b128 a[180:183], v28 offset:40960\n\t"
    "v_cvt_pk_bf16_f32 v155, v155, v159\n\t"
    "v_mfma_f32_16x16x32_bf16 v[204:207], a[148:151], a[116:119], v[204:207]\n\t"
    "buffer_load_dwordx4 v231, s[80:83], 0 offen offset:1024 lds\n\t"
    "v_mfma_f32_16x16x32_bf16 v[192:195], a[152:155], a[120:123], v[192:195]\n\t"
    "ds_read_b128 a[184:187], v29 offset:34816\n\t"
    "v_mfma_f32_16x16x32_bf16 v[196:199], a[152:155], a[124:127], v[196:199]\n\t"
    "buffer_store_dword v154, v33, s[16:19], 0 offen nt\n\t"
    "v_mul_f32 v160, v35, v160\n\t"
    "v_mfma_f32_16x16x32_bf16 v[200:203], a[156:159], a[120:123], v[200:203]\n\t"
    "ds_read_b128 a[188:191], v29 offset:40960\n\t"
    "s_waitcnt lgkmcnt(4)\n\t"
    "v_mfma_f32_16x16x32_bf16 v[204:207], a[156:159], a[124:127], v[204:207]\n\t"
    "v_mul_f32 v164, v9, v164\n\t"
    "v_cvt_pk_bf16_f32 v160, v160, v164\n\t"
    "v_mfma_f32_16x16x32_bf16 v[192:195], a[160:163], a[128:131], v[192:195]\n\t"
    "ds_read_b128 a[144:147], v26 offset:0\n\t"
    "v_mfma_f32_16x16x32_bf16 v[196:199], a[160:163], a[132:135], v[196:199]\n\t"
    "buffer_store_dword v155, v34, s[16:19], 0 offen nt\n\t"
    "v_mfma_f32_16x16x32_bf16 v[200:203], a[164:167], a[128:131], v[200:203]\n\t"
    "ds_read_b128 a[148:151], v26 offset:6144\n\t"
    "v_mul_f32 v161, v35, v161\n\t"
    "v_mfma_f32_16x16x32_bf16 v[204:207], a[164:167], a[132:135], v[204:207]\n\t"
    "buffer_load_dwordx4 v232, s[80:83], 0 offen offset:2048 lds\n\t"
    "v_mul_f32 v165, v9, v165\n\t"
    "v_mfma_f32_16x16x32_bf16 v[192:195], a[168:171], a[136:139], v[192:195]\n\t"
    "ds_read_b128 a[152:155], v27 offset:0\n\t"
    "v_cvt_pk_bf16_f32 v161, v161, v165\n\t"
    "v_mfma_f32_16x16x32_bf16 v[196:199], a[168:171], a[140:143], v[196:199]\n\t"
    "s_add_u32 s80, 73728, s84\n\t"
    "s_addc_u32 s81, s85, 0\n\t"
    "s_add_u32 m0, s75, 114688\n\t"
    "v_mfma_f32_16x16x32_bf16 v[200:203], a[172:175], a[136:139], v[200:203]\n\t"
    "ds_read_b128 a[156:159], v27 offset:6144\n\t"
    "s_waitcnt lgkmcnt(4)\n\t"
    "v_mfma_f32_16x16x32_bf16 v[204:207], a[172:175], a[140:143], v[204:207]\n\t"
    "s_add_u32 s16, 147456, s16\n\t"
    "s_addc_u32 s17, s17, 0\n\t"
    "v_mul_f32 v162, v35, v162\n\t"
    "L_pg3_%=:\n\t"
    "s_cbranch_execz L_bk3_%=\n\t"
    "v_mfma_f32_16x16x32_bf16 v[64:67], a[176:179], a[32:35], v[64:67]\n\t"
    "ds_read_b128 a[160:163], v26 offset:1024\n\t"
    "v_mul_f32 v166, v9, v166\n\t"
    "v_mfma_f32_16x16x32_bf16 v[68:71], a[176:179], a[36:39], v[68:71]\n\t"
    "buffer_store_dword v160, v4, s[16:19], 0 offen nt\n\t"
    "v_cvt_pk_bf16_f32 v162, v162, v166\n\t"
    "v_mfma_f32_16x16x32_bf16 v[72:75], a[180:183], a[32:35], v[72:75]\n\t"
    "ds_read_b128 a[164:167], v26 offset:7168\n\t"
    "v_mfma_f32_16x16x32_bf16 v[76:79], a[180:183], a[36:39], v[76:79]\n\t"
    "buffer_load_dwordx4 v226, s[80:83], 0 offen offset:2048 lds\n\t"
    "s_add_u32 m0, s75, 131072\n\t"
    "v_mfma_f32_16x16x32_bf16 v[64:67], a[184:187], a[40:43], v[64:67]\n\t"
    "ds_read_b128 a[168:171], v27 offset:1024\n\t"
    "v_mul_f32 v163, v35, v163\n\t"
    "v_mfma_f32_16x16x32_bf16 v[68:71], a[184:187], a[44:47], v[68:71]\n\t"
    "buffer_store_dword v161, v32, s[16:19], 0 offen nt\n\t"
    "v_mul_f32 v167, v9, v167\n\t"
    "v_mfma_f32_16x16x32_bf16 v[72:75], a[188:191], a[40:43], v[72:75]\n\t"
    "ds_read_b128 a[172:175], v27 offset:7168\n\t"
    "s_waitcnt lgkmcnt(4)\n\t"
    "v_mfma_f32_16x16x32_bf16 v[76:79], a[188:191], a[44:47], v[76:79]\n\t"
    "buffer_store_dword v162, v33, s[16:19], 0 offen nt\n\t"
    "v_cvt_pk_bf16_f32 v163, v163, v167\n\t"
    "v_mfma_f32_16x16x32_bf16 v[64:67], a[144:147], a[48:51], v[64:67]\n\t"
    "ds_read_b128 a[176:179], v26 offset:2048\n\t"
    "v_mfma_f32_16x16x32_bf16 v[68:71], a[144:147], a[52:55], v[68:71]\n\t"
    "v_mul_f32 v168, v35, v168\n\t"
    "v_mul_f32 v172, v9, v172\n\t"
    "v_mfma_f32_16x16x32_bf16 v[72:75], a[148:151], a[48:51], v[72:75]\n\t"
    "ds_read_b128 a[180:183], v26 offset:8192\n\t"
    "v_cvt_pk_bf16_f32 v168, v168, v172\n\t"
    "v_mfma_f32_16x16x32_bf16 v[76:79], a[148:151], a[52:55], v[76:79]\n\t"
    "buffer_load_dwordx4 v227, s[80:83], 0 offen offset:0 lds\n\t"
    "v_mfma_f32_16x16x32_bf16 v[64:67], a[152:155], a[56:59], v[64:67]\n\t"
    "ds_read_b128 a[184:187], v27 offset:2048\n\t"
    "v_mfma_f32_16x16x32_bf16 v[68:71], a[152:155], a[60:63], v[68:71]\n\t"
    "buffer_store_dword v163, v34, s[16:19], 0 offen nt\n\t"
    "v_mfma_f32_16x16x32_bf16 v[72:75], a[156:159], a[56:59], v[72:75]\n\t"
    "ds_read_b128 a[188:191], v27 offset:8192\n\t"
    "s_waitcnt lgkmcnt(4)\n\t"
    "v_mfma_f32_16x16x32_bf16 v[76:79], a[156:159], a[60:63], v[76:79]\n\t"
    "s_add_u32 s16, 147456, s16\n\t"
    "s_addc_u32 s17, s17, 0\n\t"
    "v_mul_f32 v169, v35, v169\n\t"
    "v_mfma_f32_16x16x32_bf16 v[64:67], a[160:163], a[64:67], v[64:67]\n\t"
    "ds_read_b128 a[144:147], v26 offset:12288\n\t"
    "v_mul_f32 v173, v9, v173\n\t"
    "v_mfma_f32_16x16x32_bf16 v[68:71], a[160:163], a[68:71], v[68:71]\n\t"
    "buffer_store_dword v168, v4, s[16:19], 0 offen nt\n\t"
    "v_cvt_pk_bf16_f32 v169, v169, v173\n\t"
    "v_mfma_f32_16x16x32_bf16 v[72:75], a[164:167], a[64:67], v[72:75]\n\t"
    "ds_read_b128 a[148:151], v26 offset:18432\n\t"
    "v_mfma_f32_16x16x32_bf16 v[76:79], a[164:167], a[68:71], v[76:79]\n\t"
    "buffer_load_dwordx4 v228, s[80:83], 0 offen offset:1024 lds\n\t"
    "v_mul_f32 v170, v35, v170\n\t"
    "v_mfma_f32_16x16x32_bf16 v[64:67], a[168:171], a[72:75], v[64:67]\n\t"
    "ds_read_b128 a[152:155], v27 offset:12288\n\t"
    "v_mul_f32 v174, v9, v174\n\t"
    "v_mfma_f32_16x16x32_bf16 v[68:71], a[168:171], a[76:79], v[68:71]\n\t"
    "v_cvt_pk_bf16_f32 v170, v170, v174\n\t"
    "v_mfma_f32_16x16x32_bf16 v[72:75], a[172:175], a[72:75], v[72:75]\n\t"
    "ds_read_b128 a[156:159], v27 offset:18432\n\t"
    "s_waitcnt lgkmcnt(4)\n\t"
    "v_mfma_f32_16x16x32_bf16 v[76:79], a[172:175], a[76:79], v[76:79]\n\t"
    "buffer_store_dword v169, v32, s[16:19], 0 offen nt\n\t"
    "v_mul_f32 v171, v35, v171\n\t"
    "v_mfma_f32_16x16x32_bf16 v[64:67], a[176:179], a[80:83], v[64:67]\n\t"
    "ds_read_b128 a[160:163], v26 offset:13312\n\t"
    "v_mul_f32 v175, v9, v175\n\t"
    "v_mfma_f32_16x16x32_bf16 v[68:71], a[176:179], a[84:87], v[68:71]\n\t"
    "v_cvt_pk_bf16_f32 v171, v171, v175\n\t"
    "v_mfma_f32_16x16x32_bf16 v[72:75], a[180:183], a[80:83], v[72:75]\n\t"
    "ds_read_b128 a[164:167], v26 offset:19456\n\t"
    "v_mfma_f32_16x16x32_bf16 v[76:79], a[180:183], a[84:87], v[76:79]\n\t"
    "buffer_load_dwordx4 v229, s[80:83], 0 offen offset:2048 lds\n\t"
    "s_add_u32 m0, s75, 65536\n\t"
    "v_mfma_f32_16x16x32_bf16 v[64:67], a[184:187], a[88:91], v[64:67]\n\t"
    "ds_read_b128 a[168:171], v27 offset:13312\n\t"
    "v_mul_f32 v176, v35, v176\n\t"
    "v_mfma_f32_16x16x32_bf16 v[68:71], a[184:187], a[92:95], v[68:71]\n\t"
    "buffer_store_dword v170, v33, s[16:19], 0 offen nt\n\t"
    "v_mul_f32 v180, v9, v180\n\t"
    "v_mfma_f32_16x16x32_bf16 v[72:75], a[188:191], a[88:91], v[72:75]\n\t"
    "ds_read_b128 a[172:175], v27 offset:19456\n\t"
    "s_waitcnt lgkmcnt(4)\n\t"
    "v_mfma_f32_16x16x32_bf16 v[76:79], a[188:191], a[92:95], v[76:79]\n\t"
    "buffer_store_dword v171, v34, s[16:19], 0 offen nt\n\t"
    "v_cvt_pk_bf16_f32 v176, v176, v180\n\t"
    "v_mfma_f32_16x16x32_bf16 v[64:67], a[144:147], a[96:99], v[64:67]\n\t"
    "ds_read_b128 a[176:179], v26 offset:14336\n\t"
    "v_mfma_f32_16x16x32_bf16 v[68:71], a[144:147], a[100:103], v[68:71]\n\t"
    "s_add_u32 s16, 147456, s16\n\t"
    "s_addc_u32 s17, s17, 0\n\t"
    "v_mul_f32 v177, v35, v177\n\t"
    "v_mfma_f32_16x16x32_bf16 v[72:75], a[148:151], a[96:99], v[72:75]\n\t"
    "ds_read_b128 a[180:183], v26 offset:20480\n\t"
    "v_mul_f32 v181, v9, v181\n\t"
    "v_mfma_f32_16x16x32_bf16 v[76:79], a[148:151], a[100:103], v[76:79]\n\t"
    "buffer_load_dwordx4 v230, s[80:83], 0 offen offset:0 lds\n\t"
    "v_cvt_pk_bf16_f32 v177, v177, v181\n\t"
    "v_mfma_f32_16x16x32_bf16 v[64:67], a[152:155], a[104:107], v[64:67]\n\t"
    "ds_read_b128 a[184:187], v27 offset:14336\n\t"
    "v_mfma_f32_16x16x32_bf16 v[68:71], a[152:155], a[108:111], v[68:71]\n\t"
    "buffer_store_dword v176, v4, s[16:19], 0 offen nt\n\t"
    "v_mfma_f32_16x16x32_bf16 v[72:75], a[156:159], a[104:107], v[72:75]\n\t"
    "ds_read_b128 a[188:191], v27 offset:20480\n\t"
    "s_waitcnt vmcnt(14) lgkmcnt(4)\n\t"
    "v_mfma_f32_16x16x32_bf16 v[76:79], a[156:159], a[108:111], v[76:79]\n\t"
    "s_barrier\n\t"
    "v_mul_f32 v178, v35, v178\n\t"
    "v_mul_f32 v182, v9, v182\n\t"
    "v_mfma_f32_16x16x32_bf16 v[64:67], a[160:163], a[112:115], v[64:67]\n\t"
    "ds_read_b128 a[144:147], v26 offset:26624\n\t"
    "v_cvt_pk_bf16_f32 v178, v178, v182\n\t"
    "v_mfma_f32_16x16x32_bf16 v[68:71], a[160:163], a[116:119], v[68:71]\n\t"
    "buffer_store_dword v177, v32, s[16:19], 0 offen nt\n\t"
    "v_mfma_f32_16x16x32_bf16 v[72:75], a[164:167], a[112:115], v[72:75]\n\t"
    "ds_read_b128 a[148:151], v26 offset:32768\n\t"
    "v_mfma_f32_16x16x32_bf16 v[76:79], a[164:167], a[116:119], v[76:79]\n\t"
    "buffer_load_dwordx4 v231, s[80:83], 0 offen offset:1024 lds\n\t"
    "v_mul_f32 v179, v35, v179\n\t"
    "v_mfma_f32_16x16x32_bf16 v[64:67], a[168:171], a[120:123], v[64:67]\n\t"
    "ds_read_b128 a[152:155], v27 offset:26624\n\t"
    "v_mul_f32 v183, v9, v183\n\t"
    "v_mfma_f32_16x16x32_bf16 v[68:71], a[168:171], a[124:127], v[68:71]\n\t"
    "v_cvt_pk_bf16_f32 v179, v179, v183\n\t"
    "v_mfma_f32_16x16x32_bf16 v[72:75], a[172:175], a[120:123], v[72:75]\n\t"
    "ds_read_b128 a[156:159], v27 offset:32768\n\t"
    "s_waitcnt lgkmcnt(4)\n\t"
    "v_mfma_f32_16x16x32_bf16 v[76:79], a[172:175], a[124:127], v[76:79]\n\t"
    "buffer_store_dword v178, v33, s[16:19], 0 offen nt\n\t"
    "v_mfma_f32_16x16x32_bf16 v[64:67], a[176:179], a[128:131], v[64:67]\n\t"
    "ds_read_b128 a[160:163], v26 offset:36864\n\t"
    "v_mul_f32 v184, v35, v184\n\t"
    "v_mfma_f32_16x16x32_bf16 v[68:71], a[176:179], a[132:135], v[68:71]\n\t"
    "v_mul_f32 v188, v9, v188\n\t"
    "v_cvt_pk_bf16_f32 v184, v184, v188\n\t"
    "v_mfma_f32_16x16x32_bf16 v[72:75], a[180:183], a[128:131], v[72:75]\n\t"
    "ds_read_b128 a[164:167], v26 offset:43008\n\t"
    "v_mfma_f32_16x16x32_bf16 v[76:79], a[180:183], a[132:135], v[76:79]\n\t"
    "buffer_load_dwordx4 v232, s[80:83], 0 offen offset:2048 lds\n\t"
    "v_mfma_f32_16x16x32_bf16 v[64:67], a[184:187], a[136:139], v[64:67]\n\t"
    "ds_read_b128 a[168:171], v27 offset:36864\n\t"
    "v_mfma_f32_16x16x32_bf16 v[68:71], a[184:187], a[140:143], v[68:71]\n\t"
    "s_add_u32 s80, 110592, s84\n\t"
    "s_addc_u32 s81, s85, 0\n\t"
    "s_add_u32 m0, s75, 147456\n\t"
    "v_mfma_f32_16x16x32_bf16 v[72:75], a[188:191], a[136:139], v[72:75]\n\t"
    "ds_read_b128 a[172:175], v27 offset:43008\n\t"
    "s_waitcnt lgkmcnt(4)\n\t"
    "v_mfma_f32_16x16x32_bf16 v[76:79], a[188:191], a[140:143], v[76:79]\n\t"
    "buffer_store_dword v179, v34, s[16:19], 0 offen nt\n\t"
    "v_mul_f32 v185, v35, v185\n\t"
    "v_mfma_f32_16x16x32_bf16 v[80:83], a[144:147], a[32:35], v[80:83]\n\t"
    "ds_read_b128 a[176:179], v26 offset:37888\n\t"
    "v_mul_f32 v189, v9, v189\n\t"
    "v_mfma_f32_16x16x32_bf16 v[84:87], a[144:147], a[36:39], v[84:87]\n\t"
    "s_add_u32 s16, 147456, s16\n\t"
    "s_addc_u32 s17, s17, 0\n\t"
    "v_cvt_pk_bf16_f32 v185, v185, v189\n\t"
    "v_mfma_f32_16x16x32_bf16 v[88:91], a[148:151], a[32:35], v[88:91]\n\t"
    "ds_read_b128 a[180:183], v26 offset:44032\n\t"
    "v_mul_f32 v186, v35, v186\n\t"
    "v_mfma_f32_16x16x32_bf16 v[92:95], a[148:151], a[36:39], v[92:95]\n\t"
    "buffer_load_dwordx4 v226, s[80:83], 0 offen offset:2048 lds\n\t"
    "s_add_u32 m0, s75, 0\n\t"
    "v_mfma_f32_16x16x32_bf16 v[80:83], a[152:155], a[40:43], v[80:83]\n\t"
    "ds_read_b128 a[184:187], v27 offset:37888\n\t"
    "v_mul_f32 v190, v9, v190\n\t"
    "v_mfma_f32_16x16x32_bf16 v[84:87], a[152:155], a[44:47], v[84:87]\n\t"
    "buffer_store_dword v184, v4, s[16:19], 0 offen nt\n\t"
    "v_cvt_pk_bf16_f32 v186, v186, v190\n\t"
    "v_mfma_f32_16x16x32_bf16 v[88:91], a[156:159], a[40:43], v[88:91]\n\t"
    "ds_read_b128 a[188:191], v27 offset:44032\n\t"
    "s_waitcnt lgkmcnt(4)\n\t"
    "v_mfma_f32_16x16x32_bf16 v[92:95], a[156:159], a[44:47], v[92:95]\n\t"
    "buffer_store_dword v185, v32, s[16:19], 0 offen nt\n\t"
    "v_mul_f32 v187, v35, v187\n\t"
    "v_mfma_f32_16x16x32_bf16 v[80:83], a[160:163], a[48:51], v[80:83]\n\t"
    "ds_read_b128 a[144:147], v26 offset:38912\n\t"
    "v_mul_f32 v191, v9, v191\n\t"
    "v_mfma_f32_16x16x32_bf16 v[84:87], a[160:163], a[52:55], v[84:87]\n\t"
    "buffer_store_dword v186, v33, s[16:19], 0 offen nt\n\t"
    "v_cvt_pk_bf16_f32 v187, v187, v191\n\t"
    "v_mfma_f32_16x16x32_bf16 v[88:91], a[164:167], a[48:51], v[88:91]\n\t"
    "ds_read_b128 a[148:151], v26 offset:45056\n\t"
    "v_mfma_f32_16x16x32_bf16 v[92:95], a[164:167], a[52:55], v[92:95]\n\t"
    "buffer_load_dwordx4 v227, s[80:83], 0 offen offset:0 lds\n\t"
    "v_mul_f32 v192, v35, v192\n\t"
    "v_mfma_f32_16x16x32_bf16 v[80:83], a[168:171], a[56:59], v[80:83]\n\t"
    "ds_read_b128 a[152:155], v27 offset:38912\n\t"
    "v_mul_f32 v196, v9, v196\n\t"
    "v_mfma_f32_16x16x32_bf16 v[84:87], a[168:171], a[60:63], v[84:87]\n\t"
    "v_cvt_pk_bf16_f32 v192, v192, v196\n\t"
    "v_mfma_f32_16x16x32_bf16 v[88:91], a[172:175], a[56:59], v[88:91]\n\t"
    "ds_read_b128 a[156:159], v27 offset:45056\n\t"
    "s_waitcnt lgkmcnt(4)\n\t"
    "v_mfma_f32_16x16x32_bf16 v[92:95], a[172:175], a[60:63], v[92:95]\n\t"
    "buffer_store_dword v187, v34, s[16:19], 0 offen nt\n\t"
    "v_mfma_f32_16x16x32_bf16 v[80:83], a[176:179], a[64:67], v[80:83]\n\t"
    "ds_read_b128 a[160:163], v26 offset:49152\n\t"
    "v_mul_f32 v193, v35, v193\n\t"
    "v_mfma_f32_16x16x32_bf16 v[84:87], a[176:179], a[68:71], v[84:87]\n\t"
    "s_add_u32 s16, 1179648, s90\n\t"
    "s_addc_u32 s17, s91, 0\n\t"
    "v_mul_f32 v197, v9, v197\n\t"
    "v_mfma_f32_16x16x32_bf16 v[88:91], a[180:183], a[64:67], v[88:91]\n\t"
    "ds_read_b128 a[164:167], v26 offset:55296\n\t"
    "v_cvt_pk_bf16_f32 v193, v193, v197\n\t"
    "v_mfma_f32_16x16x32_bf16 v[92:95], a[180:183], a[68:71], v[92:95]\n\t"
    "buffer_load_dwordx4 v228, s[80:83], 0 offen offset:1024 lds\n\t"
    "v_mfma_f32_16x16x32_bf16 v[80:83], a[184:187], a[72:75], v[80:83]\n\t"
    "ds_read_b128 a[168:171], v27 offset:49152\n\t"
    "v_mfma_f32_16x16x32_bf16 v[84:87], a[184:187], a[76:79], v[84:87]\n\t"
    "buffer_store_dword v192, v4, s[16:19], 0 offen nt\n\t"
    "v_mul_f32 v194, v35, v194\n\t"
    "v_mfma_f32_16x16x32_bf16 v[88:91], a[188:191], a[72:75], v[88:91]\n\t"
    "ds_read_b128 a[172:175], v27 offset:55296\n\t"
    "s_waitcnt lgkmcnt(4)\n\t"
    "v_mfma_f32_16x16x32_bf16 v[92:95], a[188:191], a[76:79], v[92:95]\n\t"
    "v_mul_f32 v198, v9, v198\n\t"
    "v_cvt_pk_bf16_f32 v194, v194, v198\n\t"
    "v_mfma_f32_16x16x32_bf16 v[80:83], a[144:147], a[80:83], v[80:83]\n\t"
    "ds_read_b128 a[176:179], v26 offset:50176\n\t"
    "v_mfma_f32_16x16x32_bf16 v[84:87], a[144:147], a[84:87], v[84:87]\n\t"
    "buffer_store_dword v193, v32, s[16:19], 0 offen nt\n\t"
    "v_mfma_f32_16x16x32_bf16 v[88:91], a[148:151], a[80:83], v[88:91]\n\t"
    "ds_read_b128 a[180:183], v26 offset:56320\n\t"
    "v_mul_f32 v195, v35, v195\n\t"
    "v_mfma_f32_16x16x32_bf16 v[92:95], a[148:151], a[84:87], v[92:95]\n\t"
    "buffer_load_dwordx4 v229, s[80:83], 0 offen offset:2048 lds\n\t"
    "s_add_u32 m0, s75, 81920\n\t"
    "v_mfma_f32_16x16x32_bf16 v[80:83], a[152:155], a[88:91], v[80:83]\n\t"
    "ds_read_b128 a[184:187], v27 offset:50176\n\t"
    "v_mul_f32 v199, v9, v199\n\t"
    "v_mfma_f32_16x16x32_bf16 v[84:87], a[152:155], a[92:95], v[84:87]\n\t"
    "buffer_store_dword v194, v33, s[16:19], 0 offen nt\n\t"
    "v_cvt_pk_bf16_f32 v195, v195, v199\n\t"
    "v_mfma_f32_16x16x32_bf16 v[88:91], a[156:159], a[88:91], v[88:91]\n\t"
    "ds_read_b128 a[188:191], v27 offset:56320\n\t"
    "s_waitcnt lgkmcnt(4)\n\t"
    "v_mfma_f32_16x16x32_bf16 v[92:95], a[156:159], a[92:95], v[92:95]\n\t"
    "v_mul_f32 v200, v35, v200\n\t"
    "v_mul_f32 v204, v9, v204\n\t"
    "v_mfma_f32_16x16x32_bf16 v[80:83], a[160:163], a[96:99], v[80:83]\n\t"
    "ds_read_b128 a[144:147], v26 offset:51200\n\t"
    "v_cvt_pk_bf16_f32 v200, v200, v204\n\t"
    "v_mfma_f32_16x16x32_bf16 v[84:87], a[160:163], a[100:103], v[84:87]\n\t"
    "v_mfma_f32_16x16x32_bf16 v[88:91], a[164:167], a[96:99], v[88:91]\n\t"
    "ds_read_b128 a[148:151], v26 offset:57344\n\t"
    "v_mfma_f32_16x16x32_bf16 v[92:95], a[164:167], a[100:103], v[92:95]\n\t"
    "buffer_load_dwordx4 v230, s[80:83], 0 offen offset:0 lds\n\t"
    "v_mul_f32 v201, v35, v201\n\t"
    "v_mfma_f32_16x16x32_bf16 v[80:83], a[168:171], a[104:107], v[80:83]\n\t"
    "ds_read_b128 a[152:155], v27 offset:51200\n\t"
    "v_mul_f32 v205, v9, v205\n\t"
    "v_mfma_f32_16x16x32_bf16 v[84:87], a[168:171], a[108:111], v[84:87]\n\t"
    "buffer_store_dword v195, v34, s[16:19], 0 offen nt\n\t"
    "v_cvt_pk_bf16_f32 v201, v201, v205\n\t"
    "v_mfma_f32_16x16x32_bf16 v[88:91], a[172:175], a[104:107], v[88:91]\n\t"
    "ds_read_b128 a[156:159], v27 offset:57344\n\t"
    "s_waitcnt vmcnt(14) lgkmcnt(4)\n\t"
    "v_mfma_f32_16x16x32_bf16 v[92:95], a[172:175], a[108:111], v[92:95]\n\t"
    "s_barrier\n\t"
    "s_add_u32 s16, 147456, s16\n\t"
    "s_addc_u32 s17, s17, 0\n\t"
    "v_mfma_f32_16x16x32_bf16 v[80:83], a[176:179], a[112:115], v[80:83]\n\t"
    "ds_read_b128 a[160:163], v28 offset:51200\n\t"
    "v_mul_f32 v202, v35, v202\n\t"
    "v_mfma_f32_16x16x32_bf16 v[84:87], a[176:179], a[116:119], v[84:87]\n\t"
    "buffer_store_dword v200, v4, s[16:19], 0 offen nt\n\t"
    "v_mul_f32 v206, v9, v206\n\t"
    "v_mfma_f32_16x16x32_bf16 v[88:91], a[180:183], a[112:115], v[88:91]\n\t"
    "ds_read_b128 a[164:167], v28 offset:57344\n\t"
    "v_cvt_pk_bf16_f32 v202, v202, v206\n\t"
    "v_mfma_f32_16x16x32_bf16 v[92:95], a[180:183], a[116:119], v[92:95]\n\t"
    "buffer_load_dwordx4 v231, s[80:83], 0 offen offset:1024 lds\n\t"
    "v_mfma_f32_16x16x32_bf16 v[80:83], a[184:187], a[120:123], v[80:83]\n\t"
    "ds_read_b128 a[168:171], v29 offset:51200\n\t"
    "v_mfma_f32_16x16x32_bf16 v[84:87], a[184:187], a[124:127], v[84:87]\n\t"
    "buffer_store_dword v201, v32, s[16:19], 0 offen nt\n\t"
    "v_mul_f32 v203, v35, v203\n\t"
    "v_mfma_f32_16x16x32_bf16 v[88:91], a[188:191], a[120:123], v[88:91]\n\t"
    "ds_read_b128 a[172:175], v29 offset:57344\n\t"
    "s_waitcnt lgkmcnt(4)\n\t"
    "v_mfma_f32_16x16x32_bf16 v[92:95], a[188:191], a[124:127], v[92:95]\n\t"
    "v_mul_f32 v207, v9, v207\n\t"
    "v_cvt_pk_bf16_f32 v203, v203, v207\n\t"
    "v_mfma_f32_16x16x32_bf16 v[80:83], a[144:147], a[128:131], v[80:83]\n\t"
    "ds_read_b128 a[176:179], v30 offset:0\n\t"
    "v_mfma_f32_16x16x32_bf16 v[84:87], a[144:147], a[132:135], v[84:87]\n\t"
    "buffer_store_dword v202, v33, s[16:19], 0 offen nt\n\t"
    "v_mfma_f32_16x16x32_bf16 v[88:91], a[148:151], a[128:131], v[88:91]\n\t"
    "ds_read_b128 a[180:183], v30 offset:6144\n\t"
    "v_mul_f32 v64, v35, v64\n\t"
    "v_mfma_f32_16x16x32_bf16 v[92:95], a[148:151], a[132:135], v[92:95]\n\t"
    "buffer_load_dwordx4 v232, s[80:83], 0 offen offset:2048 lds\n\t"
    "v_mul_f32 v68, v9, v68\n\t"
    "v_mfma_f32_16x16x32_bf16 v[80:83], a[152:155], a[136:139], v[80:83]\n\t"
    "ds_read_b128 a[184:187], v31 offset:0\n\t"
    "v_cvt_pk_bf16_f32 v64, v64, v68\n\t"
    "v_mfma_f32_16x16x32_bf16 v[84:87], a[152:155], a[140:143], v[84:87]\n\t"
    "s_add_u32 s80, 184320, s84\n\t"
    "s_addc_u32 s81, s85, 0\n\t"
    "s_mov_b32 s82, s30\n\t"
    "v_mfma_f32_16x16x32_bf16 v[88:91], a[156:159], a[136:139], v[88:91]\n\t"
    "ds_read_b128 a[188:191], v31 offset:6144\n\t"
    "s_waitcnt lgkmcnt(4)\n\t"
    "v_mfma_f32_16x16x32_bf16 v[92:95], a[156:159], a[140:143], v[92:95]\n\t"
    "buffer_store_dword v203, v34, s[16:19], 0 offen nt\n\t"
    "s_add_u32 m0, s75, 98304\n\t"
    "v_mfma_f32_16x16x32_bf16 v[96:99], a[160:163], a[32:35], v[96:99]\n\t"
    "ds_read_b128 a[144:147], v30 offset:1024\n\t"
    "v_mul_f32 v65, v35, v65\n\t"
    "v_mfma_f32_16x16x32_bf16 v[100:103], a[160:163], a[36:39], v[100:103]\n\t"
    "s_add_u32 s16, 0, s90\n\t"
    "s_addc_u32 s17, s91, 0\n\t"
    "v_mul_f32 v69, v9, v69\n\t"
    "v_mfma_f32_16x16x32_bf16 v[104:107], a[164:167], a[32:35], v[104:107]\n\t"
    "ds_read_b128 a[148:151], v30 offset:7168\n\t"
    "v_cvt_pk_bf16_f32 v65, v65, v69\n\t"
    "v_mfma_f32_16x16x32_bf16 v[108:111], a[164:167], a[36:39], v[108:111]\n\t"
    "buffer_load_dwordx4 v224, s[80:83], 0 offen offset:0 lds\n\t"
    "v_mfma_f32_16x16x32_bf16 v[96:99], a[168:171], a[40:43], v[96:99]\n\t"
    "ds_read_b128 a[152:155], v31 offset:1024\n\t"
    "v_mul_f32 v66, v35, v66\n\t"
    "v_mfma_f32_16x16x32_bf16 v[100:103], a[168:171], a[44:47], v[100:103]\n\t"
    "buffer_store_dword v64, v4, s[16:19], 0 offen nt\n\t"
    "v_mul_f32 v70, v9, v70\n\t"
    "v_mfma_f32_16x16x32_bf16 v[104:107], a[172:175], a[40:43], v[104:107]\n\t"
    "ds_read_b128 a[156:159], v31 offset:7168\n\t"
    "s_waitcnt lgkmcnt(4)\n\t"
    "v_mfma_f32_16x16x32_bf16 v[108:111], a[172:175], a[44:47], v[108:111]\n\t"
    "buffer_store_dword v65, v32, s[16:19], 0 offen nt\n\t"
    "v_cvt_pk_bf16_f32 v66, v66, v70\n\t"
    "v_mfma_f32_16x16x32_bf16 v[96:99], a[176:179], a[48:51], v[96:99]\n\t"
    "ds_read_b128 a[160:163], v30 offset:2048\n\t"
    "v_mfma_f32_16x16x32_bf16 v[100:103], a[176:179], a[52:55], v[100:103]\n\t"
    "v_mul_f32 v67, v35, v67\n\t"
    "v_mul_f32 v71, v9, v71\n\t"
    "v_mfma_f32_16x16x32_bf16 v[104:107], a[180:183], a[48:51], v[104:107]\n\t"
    "ds_read_b128 a[164:167], v30 offset:8192\n\t"
    "v_cvt_pk_bf16_f32 v67, v67, v71\n\t"
    "v_mfma_f32_16x16x32_bf16 v[108:111], a[180:183], a[52:55], v[108:111]\n\t"
    "buffer_load_dwordx4 v225, s[80:83], 0 offen offset:1024 lds\n\t"
    "v_mfma_f32_16x16x32_bf16 v[96:99], a[184:187], a[56:59], v[96:99]\n\t"
    "ds_read_b128 a[168:171], v31 offset:2048\n\t"
    "v_mfma_f32_16x16x32_bf16 v[100:103], a[184:187], a[60:63], v[100:103]\n\t"
    "buffer_store_dword v66, v33, s[16:19], 0 offen nt\n\t"
    "v_mfma_f32_16x16x32_bf16 v[104:107], a[188:191], a[56:59], v[104:107]\n\t"
    "ds_read_b128 a[172:175], v31 offset:8192\n\t"
    "s_waitcnt lgkmcnt(4)\n\t"
    "v_mfma_f32_16x16x32_bf16 v[108:111], a[188:191], a[60:63], v[108:111]\n\t"
    "v_mul_f32 v72, v35, v72\n\t"
    "v_mul_f32 v76, v9, v76\n\t"
    "v_mfma_f32_16x16x32_bf16 v[96:99], a[144:147], a[64:67], v[96:99]\n\t"
    "ds_read_b128 a[176:179], v28 offset:0\n\t"
    "v_cvt_pk_bf16_f32 v72, v72, v76\n\t"
    "v_mfma_f32_16x16x32_bf16 v[100:103], a[144:147], a[68:71], v[100:103]\n\t"
    "buffer_store_dword v67, v34, s[16:19], 0 offen nt\n\t"
    "v_mfma_f32_16x16x32_bf16 v[104:107], a[148:151], a[64:67], v[104:107]\n\t"
    "ds_read_b128 a[180:183], v28 offset:6144\n\t"
    "v_mul_f32 v73, v35, v73\n\t"
    "v_mfma_f32_16x16x32_bf16 v[108:111], a[148:151], a[68:71], v[108:111]\n\t"
    "buffer_load_dwordx4 v226, s[80:83], 0 offen offset:2048 lds\n\t"
    "s_add_u32 m0, s75, 12288\n\t"
    "v_mfma_f32_16x16x32_bf16 v[96:99], a[152:155], a[72:75], v[96:99]\n\t"
    "ds_read_b128 a[184:187], v29 offset:0\n\t"
    "v_mul_f32 v77, v9, v77\n\t"
    "v_mfma_f32_16x16x32_bf16 v[100:103], a[152:155], a[76:79], v[100:103]\n\t"
    "s_add_u32 s16, 147456, s16\n\t"
    "s_addc_u32 s17, s17, 0\n\t"
    "v_cvt_pk_bf16_f32 v73, v73, v77\n\t"
    "v_mfma_f32_16x16x32_bf16 v[104:107], a[156:159], a[72:75], v[104:107]\n\t"
    "ds_read_b128 a[188:191], v29 offset:6144\n\t"
    "s_waitcnt lgkmcnt(4)\n\t"
    "v_mfma_f32_16x16x32_bf16 v[108:111], a[156:159], a[76:79], v[108:111]\n\t"
    "buffer_store_dword v72, v4, s[16:19], 0 offen nt\n\t"
    "v_mul_f32 v74, v35, v74\n\t"
    "v_mfma_f32_16x16x32_bf16 v[96:99], a[160:163], a[80:83], v[96:99]\n\t"
    "ds_read_b128 a[144:147], v28 offset:1024\n\t"
    "v_mul_f32 v78, v9, v78\n\t"
    "v_mfma_f32_16x16x32_bf16 v[100:103], a[160:163], a[84:87], v[100:103]\n\t"
    "v_cvt_pk_bf16_f32 v74, v74, v78\n\t"
    "v_mfma_f32_16x16x32_bf16 v[104:107], a[164:167], a[80:83], v[104:107]\n\t"
    "ds_read_b128 a[148:151], v28 offset:7168\n\t"
    "v_mfma_f32_16x16x32_bf16 v[108:111], a[164:167], a[84:87], v[108:111]\n\t"
    "buffer_load_dwordx4 v227, s[80:83], 0 offen offset:0 lds\n\t"
    "v_mfma_f32_16x16x32_bf16 v[96:99], a[168:171], a[88:91], v[96:99]\n\t"
    "ds_read_b128 a[152:155], v29 offset:1024\n\t"
    "v_mul_f32 v75, v35, v75\n\t"
    "v_mfma_f32_16x16x32_bf16 v[100:103], a[168:171], a[92:95], v[100:103]\n\t"
    "buffer_store_dword v73, v32, s[16:19], 0 offen nt\n\t"
    "v_mul_f32 v79, v9, v79\n\t"
    "v_mfma_f32_16x16x32_bf16 v[104:107], a[172:175], a[88:91], v[104:107]\n\t"
    "ds_read_b128 a[156:159], v29 offset:7168\n\t"
    "s_waitcnt lgkmcnt(4)\n\t"
    "v_mfma_f32_16x16x32_bf16 v[108:111], a[172:175], a[92:95], v[108:111]\n\t"
    "buffer_store_dword v74, v33, s[16:19], 0 offen nt\n\t"
    "v_cvt_pk_bf16_f32 v75, v75, v79\n\t"
    "v_mfma_f32_16x16x32_bf16 v[96:99], a[176:179], a[96:99], v[96:99]\n\t"
    "ds_read_b128 a[160:163], v28 offset:2048\n\t"
    "v_mfma_f32_16x16x32_bf16 v[100:103], a[176:179], a[100:103], v[100:103]\n\t"
    "v_mul_f32 v80, v35, v80\n\t"
    "v_mul_f32 v84, v9, v84\n\t"
    "v_mfma_f32_16x16x32_bf16 v[104:107], a[180:183], a[96:99], v[104:107]\n\t"
    "ds_read_b128 a[164:167], v28 offset:8192\n\t"
    "v_cvt_pk_bf16_f32 v80, v80, v84\n\t"
    "v_mfma_f32_16x16x32_bf16 v[108:111], a[180:183], a[100:103], v[108:111]\n\t"
    "buffer_load_dwordx4 v228, s[80:83], 0 offen offset:1024 lds\n\t"
    "v_mfma_f32_16x16x32_bf16 v[96:99], a[184:187], a[104:107], v[96:99]\n\t"
    "ds_read_b128 a[168:171], v29 offset:2048\n\t"
    "v_mfma_f32_16x16x32_bf16 v[100:103], a[184:187], a[108:111], v[100:103]\n\t"
    "buffer_store_dword v75, v34, s[16:19], 0 offen nt\n\t"
    "v_mfma_f32_16x16x32_bf16 v[104:107], a[188:191], a[104:107], v[104:107]\n\t"
    "ds_read_b128 a[172:175], v29 offset:8192\n\t"
    "s_waitcnt vmcnt(14) lgkmcnt(4)\n\t"
    "v_mfma_f32_16x16x32_bf16 v[108:111], a[188:191], a[108:111], v[108:111]\n\t"
    "s_barrier\n\t"
    "s_add_u32 s16, 294912, s90\n\t"
    "s_addc_u32 s17, s91, 0\n\t"
    "v_mfma_f32_16x16x32_bf16 v[96:99], a[144:147], a[112:115], v[96:99]\n\t"
    "ds_read_b128 a[176:179], v30 offset:18432\n\t"
    "v_mul_f32 v81, v35, v81\n\t"
    "v_mfma_f32_16x16x32_bf16 v[100:103], a[144:147], a[116:119], v[100:103]\n\t"
    "buffer_store_dword v80, v4, s[16:19], 0 offen nt\n\t"
    "v_mul_f32 v85, v9, v85\n\t"
    "v_mfma_f32_16x16x32_bf16 v[104:107], a[148:151], a[112:115], v[104:107]\n\t"
    "ds_read_b128 a[180:183], v30 offset:24576\n\t"
    "v_cvt_pk_bf16_f32 v81, v81, v85\n\t"
    "v_mfma_f32_16x16x32_bf16 v[108:111], a[148:151], a[116:119], v[108:111]\n\t"
    "buffer_load_dwordx4 v229, s[80:83], 0 offen offset:2048 lds\n\t"
    "s_add_u32 m0, s75, 24576\n\t"
    "v_mfma_f32_16x16x32_bf16 v[96:99], a[152:155], a[120:123], v[96:99]\n\t"
    "ds_read_b128 a[184:187], v31 offset:18432\n\t"
    "v_mul_f32 v82, v35, v82\n\t"
    "v_mfma_f32_16x16x32_bf16 v[100:103], a[152:155], a[124:127], v[100:103]\n\t"
    "v_mul_f32 v86, v9, v86\n\t"
    "v_cvt_pk_bf16_f32 v82, v82, v86\n\t"
    "v_mfma_f32_16x16x32_bf16 v[104:107], a[156:159], a[120:123], v[104:107]\n\t"
    "ds_read_b128 a[188:191], v31 offset:24576\n\t"
    "s_waitcnt lgkmcnt(4)\n\t"
    "v_mfma_f32_16x16x32_bf16 v[108:111], a[156:159], a[124:127], v[108:111]\n\t"
    "buffer_store_dword v81, v32, s[16:19], 0 offen nt\n\t"
    "v_mfma_f32_16x16x32_bf16 v[96:99], a[160:163], a[128:131], v[96:99]\n\t"
    "ds_read_b128 a[144:147], v26 offset:0\n\t"
    "v_mul_f32 v83, v35, v83\n\t"
    "v_mfma_f32_16x16x32_bf16 v[100:103], a[160:163], a[132:135], v[100:103]\n\t"
    "v_mul_f32 v87, v9, v87\n\t"
    "v_cvt_pk_bf16_f32 v83, v83, v87\n\t"
    "v_mfma_f32_16x16x32_bf16 v[104:107], a[164:167], a[128:131], v[104:107]\n\t"
    "ds_read_b128 a[148:151], v26 offset:6144\n\t"
    "v_mfma_f32_16x16x32_bf16 v[108:111], a[164:167], a[132:135], v[108:111]\n\t"
    "buffer_load_dwordx4 v230, s[80:83], 0 offen offset:0 lds\n\t"
    "v_mfma_f32_16x16x32_bf16 v[96:99], a[168:171], a[136:139], v[96:99]\n\t"
    "ds_read_b128 a[152:155], v27 offset:0\n\t"
    "v_mul_f32 v88, v35, v88\n\t"
    "v_mfma_f32_16x16x32_bf16 v[100:103], a[168:171], a[140:143], v[100:103]\n\t"
    "buffer_store_dword v82, v33, s[16:19], 0 offen nt\n\t"
    "v_mul_f32 v92, v9, v92\n\t"
    "v_mfma_f32_16x16x32_bf16 v[104:107], a[172:175], a[136:139], v[104:107]\n\t"
    "ds_read_b128 a[156:159], v27 offset:6144\n\t"
    "s_waitcnt lgkmcnt(4)\n\t"
    "v_mfma_f32_16x16x32_bf16 v[108:111], a[172:175], a[140:143], v[108:111]\n\t"
    "v_cvt_pk_bf16_f32 v88, v88, v92\n\t"
    "v_mfma_f32_16x16x32_bf16 v[112:115], a[176:179], a[32:35], v[112:115]\n\t"
    "ds_read_b128 a[160:163], v26 offset:1024\n\t"
    "v_mfma_f32_16x16x32_bf16 v[116:119], a[176:179], a[36:39], v[116:119]\n\t"
    "buffer_store_dword v83, v34, s[16:19], 0 offen nt\n\t"
    "v_mul_f32 v89, v35, v89\n\t"
    "v_mfma_f32_16x16x32_bf16 v[120:123], a[180:183], a[32:35], v[120:123]\n\t"
    "ds_read_b128 a[164:167], v26 offset:7168\n\t"
    "v_mul_f32 v93, v9, v93\n\t"
    "v_mfma_f32_16x16x32_bf16 v[124:127], a[180:183], a[36:39], v[124:127]\n\t"
    "buffer_load_dwordx4 v231, s[80:83], 0 offen offset:1024 lds\n\t"
    "v_cvt_pk_bf16_f32 v89, v89, v93\n\t"
    "v_mfma_f32_16x16x32_bf16 v[112:115], a[184:187], a[40:43], v[112:115]\n\t"
    "ds_read_b128 a[168:171], v27 offset:1024\n\t"
    "v_mfma_f32_16x16x32_bf16 v[116:119], a[184:187], a[44:47], v[116:119]\n\t"
    "s_add_u32 s16, 147456, s16\n\t"
    "s_addc_u32 s17, s17, 0\n\t"
    "v_mfma_f32_16x16x32_bf16 v[120:123], a[188:191], a[40:43], v[120:123]\n\t"
    "ds_read_b128 a[172:175], v27 offset:7168\n\t"
    "s_waitcnt lgkmcnt(4)\n\t"
    "v_mfma_f32_16x16x32_bf16 v[124:127], a[188:191], a[44:47], v[124:127]\n\t"
    "buffer_store_dword v88, v4, s[16:19], 0 offen nt\n\t"
    "v_mul_f32 v90, v35, v90\n\t"
    "v_mfma_f32_16x16x32_bf16 v[112:115], a[144:147], a[48:51], v[112:115]\n\t"
    "ds_read_b128 a[176:179], v26 offset:2048\n\t"
    "v_mul_f32 v94, v9, v94\n\t"
    "v_mfma_f32_16x16x32_bf16 v[116:119], a[144:147], a[52:55], v[116:119]\n\t"
    "buffer_store_dword v89, v32, s[16:19], 0 offen nt\n\t"
    "v_cvt_pk_bf16_f32 v90, v90, v94\n\t"
    "v_mfma_f32_16x16x32_bf16 v[120:123], a[148:151], a[48:51], v[120:123]\n\t"
    "ds_read_b128 a[180:183], v26 offset:8192\n\t"
    "v_mfma_f32_16x16x32_bf16 v[124:127], a[148:151], a[52:55], v[124:127]\n\t"
    "buffer_load_dwordx4 v232, s[80:83], 0 offen offset:2048 lds\n\t"
    "v_mul_f32 v91, v35, v91\n\t"
    "v_mfma_f32_16x16x32_bf16 v[112:115], a[152:155], a[56:59], v[112:115]\n\t"
    "ds_read_b128 a[184:187], v27 offset:2048\n\t"
    "v_mul_f32 v95, v9, v95\n\t"
    "v_mfma_f32_16x16x32_bf16 v[116:119], a[152:155], a[60:63], v[116:119]\n\t"
    "v_cvt_pk_bf16_f32 v91, v91, v95\n\t"
    "v_mfma_f32_16x16x32_bf16 v[120:123], a[156:159], a[56:59], v[120:123]\n\t"
    "ds_read_b128 a[188:191], v27 offset:8192\n\t"
    "s_waitcnt lgkmcnt(4)\n\t"
    "v_mfma_f32_16x16x32_bf16 v[124:127], a[156:159], a[60:63], v[124:127]\n\t"
    "buffer_store_dword v90, v33, s[16:19], 0 offen nt\n\t"
    "v_mfma_f32_16x16x32_bf16 v[112:115], a[160:163], a[64:67], v[112:115]\n\t"
    "ds_read_b128 a[144:147], v28 offset:16384\n\t"
    "v_mul_f32 v96, v35, v96\n\t"
    "v_mfma_f32_16x16x32_bf16 v[116:119], a[160:163], a[68:71], v[116:119]\n\t"
    "v_mul_f32 v100, v9, v100\n\t"
    "v_cvt_pk_bf16_f32 v96, v96, v100\n\t"
    "v_mfma_f32_16x16x32_bf16 v[120:123], a[164:167], a[64:67], v[120:123]\n\t"
    "ds_read_b128 a[148:151], v28 offset:22528\n\t"
    "v_mfma_f32_16x16x32_bf16 v[124:127], a[164:167], a[68:71], v[124:127]\n\t"
    "buffer_store_dword v91, v34, s[16:19], 0 offen nt\n\t"
    "v_mfma_f32_16x16x32_bf16 v[112:115], a[168:171], a[72:75], v[112:115]\n\t"
    "ds_read_b128 a[152:155], v29 offset:16384\n\t"
    "v_mfma_f32_16x16x32_bf16 v[116:119], a[168:171], a[76:79], v[116:119]\n\t"
    "s_add_u32 s16, 589824, s90\n\t"
    "s_addc_u32 s17, s91, 0\n\t"
    "v_mul_f32 v97, v35, v97\n\t"
    "v_mfma_f32_16x16x32_bf16 v[120:123], a[172:175], a[72:75], v[120:123]\n\t"
    "ds_read_b128 a[156:159], v29 offset:22528\n\t"
    "s_waitcnt lgkmcnt(4)\n\t"
    "v_mfma_f32_16x16x32_bf16 v[124:127], a[172:175], a[76:79], v[124:127]\n\t"
    "v_mul_f32 v101, v9, v101\n\t"
    "v_cvt_pk_bf16_f32 v97, v97, v101\n\t"
    "v_mfma_f32_16x16x32_bf16 v[112:115], a[176:179], a[80:83], v[112:115]\n\t"
    "ds_read_b128 a[160:163], v28 offset:17408\n\t"
    "v_mfma_f32_16x16x32_bf16 v[116:119], a[176:179], a[84:87], v[116:119]\n\t"
    "buffer_store_dword v96, v4, s[16:19], 0 offen nt\n\t"
    "v_mfma_f32_16x16x32_bf16 v[120:123], a[180:183], a[80:83], v[120:123]\n\t"
    "ds_read_b128 a[164:167], v28 offset:23552\n\t"
    "v_mul_f32 v98, v35, v98\n\t"
    "v_mfma_f32_16x16x32_bf16 v[124:127], a[180:183], a[84:87], v[124:127]\n\t"
    "v_mul_f32 v102, v9, v102\n\t"
    "v_cvt_pk_bf16_f32 v98, v98, v102\n\t"
    "v_mfma_f32_16x16x32_bf16 v[112:115], a[184:187], a[88:91], v[112:115]\n\t"
    "ds_read_b128 a[168:171], v29 offset:17408\n\t"
    "v_mfma_f32_16x16x32_bf16 v[116:119], a[184:187], a[92:95], v[116:119]\n\t"
    "buffer_store_dword v97, v32, s[16:19], 0 offen nt\n\t"
    "v_mfma_f32_16x16x32_bf16 v[120:123], a[188:191], a[88:91], v[120:123]\n\t"
    "ds_read_b128 a[172:175], v29 offset:23552\n\t"
    "s_waitcnt lgkmcnt(4)\n\t"
    "v_mfma_f32_16x16x32_bf16 v[124:127], a[188:191], a[92:95], v[124:127]\n\t"
    "v_mul_f32 v99, v35, v99\n\t"
    "v_mul_f32 v103, v9, v103\n\t"
    "v_mfma_f32_16x16x32_bf16 v[112:115], a[144:147], a[96:99], v[112:115]\n\t"
    "ds_read_b128 a[176:179], v28 offset:18432\n\t"
    "v_cvt_pk_bf16_f32 v99, v99, v103\n\t"
    "v_mfma_f32_16x16x32_bf16 v[116:119], a[144:147], a[100:103], v[116:119]\n\t"
    "v_mfma_f32_16x16x32_bf16 v[120:123], a[148:151], a[96:99], v[120:123]\n\t"
    "ds_read_b128 a[180:183], v28 offset:24576\n\t"
    "v_mfma_f32_16x16x32_bf16 v[124:127], a[148:151], a[100:103], v[124:127]\n\t"
    "buffer_store_dword v98, v33, s[16:19], 0 offen nt\n\t"
    "v_mul_f32 v104, v35, v104\n\t"
    "v_mfma_f32_16x16x32_bf16 v[112:115], a[152:155], a[104:107], v[112:115]\n\t"
    "ds_read_b128 a[184:187], v29 offset:18432\n\t"
    "v_mul_f32 v108, v9, v108\n\t"
    "v_mfma_f32_16x16x32_bf16 v[116:119], a[152:155], a[108:111], v[116:119]\n\t"
    "v_cvt_pk_bf16_f32 v104, v104, v108\n\t"
    "v_mfma_f32_16x16x32_bf16 v[120:123], a[156:159], a[104:107], v[120:123]\n\t"
    "ds_read_b128 a[188:191], v29 offset:24576\n\t"
    "s_waitcnt vmcnt(5) lgkmcnt(4)\n\t"
    "v_mfma_f32_16x16x32_bf16 v[124:127], a[156:159], a[108:111], v[124:127]\n\t"
    "buffer_store_dword v99, v34, s[16:19], 0 offen nt\n\t"
    "s_barrier\n\t"
    "v_mfma_f32_16x16x32_bf16 v[112:115], a[160:163], a[112:115], v[112:115]\n\t"
    "ds_read_b128 a[144:147], v28 offset:32768\n\t"
    "v_mul_f32 v105, v35, v105\n\t"
    "v_mfma_f32_16x16x32_bf16 v[116:119], a[160:163], a[116:119], v[116:119]\n\t"
    "s_add_u32 s16, 147456, s16\n\t"
    "s_addc_u32 s17, s17, 0\n\t"
    "v_mul_f32 v109, v9, v109\n\t"
    "v_mfma_f32_16x16x32_bf16 v[120:123], a[164:167], a[112:115], v[120:123]\n\t"
    "ds_read_b128 a[148:151], v29 offset:32768\n\t"
    "v_cvt_pk_bf16_f32 v105, v105, v109\n\t"
    "v_mfma_f32_16x16x32_bf16 v[124:127], a[164:167], a[116:119], v[124:127]\n\t"
    "buffer_store_dword v104, v4, s[16:19], 0 offen nt\n\t"
    "L_pg4_%=:\n\t"
    "s_cbranch_execz L_bk4_%=\n\t"
    "v_mfma_f32_16x16x32_bf16 v[112:115], a[168:171], a[120:123], v[112:115]\n\t"
    "ds_read_b128 a[152:155], v28 offset:33792\n\t"
    "v_mfma_f32_16x16x32_bf16 v[116:119], a[168:171], a[124:127], v[116:119]\n\t"
    "v_mul_f32 v106, v35, v106\n\t"
    "v_mul_f32 v110, v9, v110\n\t"
    "v_mfma_f32_16x16x32_bf16 v[120:123], a[172:175], a[120:123], v[120:123]\n\t"
    "ds_read_b128 a[156:159], v29 offset:33792\n\t"
    "s_waitcnt lgkmcnt(4)\n\t"
    "v_mfma_f32_16x16x32_bf16 v[124:127], a[172:175], a[124:127], v[124:127]\n\t"
    "v_cvt_pk_bf16_f32 v106, v106, v110\n\t"
    "v_mfma_f32_16x16x32_bf16 v[112:115], a[176:179], a[128:131], v[112:115]\n\t"
    "ds_read_b128 a[160:163], v28 offset:34816\n\t"
    "v_mfma_f32_16x16x32_bf16 v[116:119], a[176:179], a[132:135], v[116:119]\n\t"
    "buffer_store_dword v105, v32, s[16:19], 0 offen nt\n\t"
    "v_mfma_f32_16x16x32_bf16 v[120:123], a[180:183], a[128:131], v[120:123]\n\t"
    "ds_read_b128 a[164:167], v29 offset:34816\n\t"
    "v_mul_f32 v107, v35, v107\n\t"
    "v_mfma_f32_16x16x32_bf16 v[124:127], a[180:183], a[132:135], v[124:127]\n\t"
    "v_mul_f32 v111, v9, v111\n\t"
    "v_cvt_pk_bf16_f32 v107, v107, v111\n\t"
    "v_mfma_f32_16x16x32_bf16 v[112:115], a[184:187], a[136:139], v[112:115]\n\t"
    "ds_read_b128 a[168:171], v26 offset:12288\n\t"
    "v_mfma_f32_16x16x32_bf16 v[116:119], a[184:187], a[140:143], v[116:119]\n\t"
    "buffer_store_dword v106, v33, s[16:19], 0 offen nt\n\t"
    "v_mfma_f32_16x16x32_bf16 v[120:123], a[188:191], a[136:139], v[120:123]\n\t"
    "ds_read_b128 a[172:175], v27 offset:12288\n\t"
    "s_waitcnt lgkmcnt(4)\n\t"
    "v_mfma_f32_16x16x32_bf16 v[124:127], a[188:191], a[140:143], v[124:127]\n\t"
    "s_cmp_eq_u32 s28, 0\n\t"
    "s_cbranch_scc1 L_tail18_%=\n\t"
    "v_mfma_f32_16x16x32_bf16 v[208:211], a[144:147], a[0:3], 0\n\t"
    "ds_read_b128 a[176:179], v26 offset:13312\n\t"
    "v_mfma_f32_16x16x32_bf16 v[212:215], a[144:147], a[4:7], 0\n\t"
    "v_mfma_f32_16x16x32_bf16 v[208:211], a[148:151], a[8:11], v[208:211]\n\t"
    "ds_read_b128 a[180:183], v27 offset:13312\n\t"
    "v_mul_f32 v112, v35, v112\n\t"
    "v_mfma_f32_16x16x32_bf16 v[212:215], a[148:151], a[12:15], v[212:215]\n\t"
    "buffer_store_dword v107, v34, s[16:19], 0 offen nt\n\t"
    "v_mul_f32 v116, v9, v116\n\t"
    "v_mfma_f32_16x16x32_bf16 v[208:211], a[152:155], a[16:19], v[208:211]\n\t"
    "ds_read_b128 a[184:187], v26 offset:14336\n\t"
    "v_cvt_pk_bf16_f32 v112, v112, v116\n\t"
    "v_mfma_f32_16x16x32_bf16 v[212:215], a[152:155], a[20:23], v[212:215]\n\t"
    "s_add_u32 s16, 884736, s90\n\t"
    "s_addc_u32 s17, s91, 0\n\t"
    "v_mul_f32 v113, v35, v113\n\t"
    "v_mfma_f32_16x16x32_bf16 v[208:211], a[156:159], a[24:27], v[208:211]\n\t"
    "ds_read_b128 a[188:191], v27 offset:14336\n\t"
    "s_waitcnt lgkmcnt(4)\n\t"
    "v_mfma_f32_16x16x32_bf16 v[212:215], a[156:159], a[28:31], v[212:215]\n\t"
    "buffer_store_dword v112, v4, s[16:19], 0 offen nt\n\t"
    "v_mul_f32 v117, v9, v117\n\t"
    "v_mfma_f32_16x16x32_bf16 v[208:211], a[160:163], a[32:35], v[208:211]\n\t"
    "ds_read_b128 a[144:147], v26 offset:24576\n\t"
    "v_cvt_pk_bf16_f32 v113, v113, v117\n\t"
    "v_mfma_f32_16x16x32_bf16 v[212:215], a[160:163], a[36:39], v[212:215]\n\t"
    "v_mul_f32 v114, v35, v114\n\t"
    "v_mul_f32 v118, v9, v118\n\t"
    "v_mfma_f32_16x16x32_bf16 v[208:211], a[164:167], a[40:43], v[208:211]\n\t"
    "ds_read_b128 a[148:151], v27 offset:24576\n\t"
    "v_cvt_pk_bf16_f32 v114, v114, v118\n\t"
    "v_mfma_f32_16x16x32_bf16 v[212:215], a[164:167], a[44:47], v[212:215]\n\t"
    "v_mfma_f32_16x16x32_bf16 v[208:211], a[168:171], a[48:51], v[208:211]\n\t"
    "ds_read_b128 a[152:155], v26 offset:25600\n\t"
    "v_mfma_f32_16x16x32_bf16 v[212:215], a[168:171], a[52:55], v[212:215]\n\t"
    "buffer_store_dword v113, v32, s[16:19], 0 offen nt\n\t"
    "v_mul_f32 v115, v35, v115\n\t"
    "v_mfma_f32_16x16x32_bf16 v[208:211], a[172:175], a[56:59], v[208:211]\n\t"
    "ds_read_b128 a[156:159], v27 offset:25600\n\t"
    "s_waitcnt lgkmcnt(4)\n\t"
    "v_mfma_f32_16x16x32_bf16 v[212:215], a[172:175], a[60:63], v[212:215]\n\t"
    "v_mul_f32 v119, v9, v119\n\t"
    "v_cvt_pk_bf16_f32 v115, v115, v119\n\t"
    "v_mfma_f32_16x16x32_bf16 v[208:211], a[176:179], a[64:67], v[208:211]\n\t"
    "ds_read_b128 a[160:163], v26 offset:26624\n\t"
    "v_mfma_f32_16x16x32_bf16 v[212:215], a[176:179], a[68:71], v[212:215]\n\t"
    "buffer_store_dword v114, v33, s[16:19], 0 offen nt\n\t"
    "v_mfma_f32_16x16x32_bf16 v[208:211], a[180:183], a[72:75], v[208:211]\n\t"
    "ds_read_b128 a[164:167], v27 offset:26624\n\t"
    "v_mul_f32 v120, v35, v120\n\t"
    "v_mfma_f32_16x16x32_bf16 v[212:215], a[180:183], a[76:79], v[212:215]\n\t"
    "v_mul_f32 v124, v9, v124\n\t"
    "v_cvt_pk_bf16_f32 v120, v120, v124\n\t"
    "v_mfma_f32_16x16x32_bf16 v[208:211], a[184:187], a[80:83], v[208:211]\n\t"
    "v_mfma_f32_16x16x32_bf16 v[212:215], a[184:187], a[84:87], v[212:215]\n\t"
    "buffer_store_dword v115, v34, s[16:19], 0 offen nt\n\t"
    "v_mfma_f32_16x16x32_bf16 v[208:211], a[188:191], a[88:91], v[208:211]\n\t"
    "s_waitcnt lgkmcnt(2)\n\t"
    "s_add_u32 s16, 147456, s16\n\t"
    "s_addc_u32 s17, s17, 0\n\t"
    "v_mfma_f32_16x16x32_bf16 v[212:215], a[188:191], a[92:95], v[212:215]\n\t"
    "v_mul_f32 v121, v35, v121\n\t"
    "v_mul_f32 v125, v9, v125\n\t"
    "v_mfma_f32_16x16x32_bf16 v[208:211], a[144:147], a[96:99], v[208:211]\n\t"
    "v_cvt_pk_bf16_f32 v121, v121, v125\n\t"
    "v_mfma_f32_16x16x32_bf16 v[212:215], a[144:147], a[100:103], v[212:215]\n\t"
    "v_mfma_f32_16x16x32_bf16 v[208:211], a[148:151], a[104:107], v[208:211]\n\t"
    "buffer_store_dword v120, v4, s[16:19], 0 offen nt\n\t"
    "v_mfma_f32_16x16x32_bf16 v[212:215], a[148:151], a[108:111], v[212:215]\n\t"
    "v_mfma_f32_16x16x32_bf16 v[208:211], a[152:155], a[112:115], v[208:211]\n\t"
    "v_mul_f32 v122, v35, v122\n\t"
    "v_mul_f32 v126, v9, v126\n\t"
    "v_mfma_f32_16x16x32_bf16 v[212:215], a[152:155], a[116:119], v[212:215]\n\t"
    "v_cvt_pk_bf16_f32 v122, v122, v126\n\t"
    "v_mfma_f32_16x16x32_bf16 v[208:211], a[156:159], a[120:123], v[208:211]\n\t"
    "s_waitcnt lgkmcnt(0)\n\t"
    "v_mfma_f32_16x16x32_bf16 v[212:215], a[156:159], a[124:127], v[212:215]\n\t"
    "buffer_store_dword v121, v32, s[16:19], 0 offen nt\n\t"
    "v_mfma_f32_16x16x32_bf16 v[208:211], a[160:163], a[128:131], v[208:211]\n\t"
    "v_mfma_f32_16x16x32_bf16 v[212:215], a[160:163], a[132:135], v[212:215]\n\t"
    "v_mul_f32 v123, v35, v123\n\t"
    "v_mul_f32 v127, v9, v127\n\t"
    "v_mfma_f32_16x16x32_bf16 v[208:211], a[164:167], a[136:139], v[208:211]\n\t"
    "v_cvt_pk_bf16_f32 v123, v123, v127\n\t"
    "v_mfma_f32_16x16x32_bf16 v[212:215], a[164:167], a[140:143], v[212:215]\n\t"
    "buffer_store_dword v122, v33, s[16:19], 0 offen nt\n\t"
    "buffer_store_dword v123, v34, s[16:19], 0 offen nt\n\t"
    "s_nop 3\n\t"
    "v_mul_f32 v208, v35, v208\n\t"
    "s_nop 0\n\t"
    "v_mul_f32 v212, v9, v212\n\t"
    "v_cvt_pk_bf16_f32 v208, v208, v212\n\t"
    "s_add_u32 s16, 1474560, s90\n\t"
    "s_addc_u32 s17, s91, 0\n\t"
    "buffer_store_dword v208, v4, s[16:19], 0 offen nt\n\t"
    "v_mul_f32 v209, v35, v209\n\t"
    "v_mul_f32 v213, v9, v213\n\t"
    "v_cvt_pk_bf16_f32 v209, v209, v213\n\t"
    "buffer_store_dword v209, v32, s[16:19], 0 offen nt\n\t"
    "v_mul_f32 v210, v35, v210\n\t"
    "v_mul_f32 v214, v9, v214\n\t"
    "v_cvt_pk_bf16_f32 v210, v210, v214\n\t"
    "buffer_store_dword v210, v33, s[16:19], 0 offen nt\n\t"
    "v_mul_f32 v211, v35, v211\n\t"
    "v_mul_f32 v215, v9, v215\n\t"
    "v_cvt_pk_bf16_f32 v211, v211, v215\n\t"
    "buffer_store_dword v211, v34, s[16:19], 0 offen nt\n\t"
    "s_endpgm\n\t"
    "L_tail18_%=:\n\t"
    "buffer_store_dword v107, v34, s[16:19], 0 offen nt\n\t"
    "v_mul_f32 v112, v35, v112\n\t"
    "v_mul_f32 v116, v9, v116\n\t"
    "v_cvt_pk_bf16_f32 v112, v112, v116\n\t"
    "s_add_u32 s16, 884736, s90\n\t"
    "s_addc_u32 s17, s91, 0\n\t"
    "buffer_store_dword v112, v4, s[16:19], 0 offen nt\n\t"
    "v_mul_f32 v113, v35, v113\n\t"
    "v_mul_f32 v117, v9, v117\n\t"
    "v_cvt_pk_bf16_f32 v113, v113, v117\n\t"
    "buffer_store_dword v113, v32, s[16:19], 0 offen nt\n\t"
    "v_mul_f32 v114, v35, v114\n\t"
    "v_mul_f32 v118, v9, v118\n\t"
    "v_cvt_pk_bf16_f32 v114, v114, v118\n\t"
    "buffer_store_dword v114, v33, s[16:19], 0 offen nt\n\t"
    "v_mul_f32 v115, v35, v115\n\t"
    "v_mul_f32 v119, v9, v119\n\t"
    "v_cvt_pk_bf16_f32 v115, v115, v119\n\t"
    "buffer_store_dword v115, v34, s[16:19], 0 offen nt\n\t"
    "v_mul_f32 v120, v35, v120\n\t"
    "v_mul_f32 v124, v9, v124\n\t"
    "v_cvt_pk_bf16_f32 v120, v120, v124\n\t"
    "s_add_u32 s16, 147456, s16\n\t"
    "s_addc_u32 s17, s17, 0\n\t"
    "buffer_store_dword v120, v4, s[16:19], 0 offen nt\n\t"
    "v_mul_f32 v121, v35, v121\n\t"
    "v_mul_f32 v125, v9, v125\n\t"
    "v_cvt_pk_bf16_f32 v121, v121, v125\n\t"
    "buffer_store_dword v121, v32, s[16:19], 0 offen nt\n\t"
    "v_mul_f32 v122, v35, v122\n\t"
    "v_mul_f32 v126, v9, v126\n\t"
    "v_cvt_pk_bf16_f32 v122, v122, v126\n\t"
    "buffer_store_dword v122, v33, s[16:19], 0 offen nt\n\t"
    "v_mul_f32 v123, v35, v123\n\t"
    "v_mul_f32 v127, v9, v127\n\t"
    "v_cvt_pk_bf16_f32 v123, v123, v127\n\t"
    "buffer_store_dword v123, v34, s[16:19], 0 offen nt\n\t"
    "L_end_%=:\n\t"
    "s_endpgm\n\t"
    "L_spin0_%=:\n\t"
    "s_sub_u32 s33, s33, 1\n\t"
    "s_cbranch_scc1 L_fb0_%=\n\t"
    "buffer_load_dword v22, v23, s[92:95], 0 offen sc1\n\t"
    "s_waitcnt vmcnt(0)\n\t"
    "s_branch L_chk0_%=\n\t"
    "L_spin1_%=:\n\t"
    "s_sub_u32 s33, s33, 1\n\t"
    "s_cbranch_scc1 L_fb1_%=\n\t"
    "buffer_load_dword v22, v23, s[92:95], 0 offen sc1\n\t"
    "s_waitcnt vmcnt(0)\n\t"
    "s_branch L_chk1_%=\n\t"
    "L_fb0_%=:\n\t"
    "s_mov_b32 s33, 0\n\t"
    "s_mov_b64 s[96:97], vcc\n\t"
    "s_mul_i32 s29, s2, 36864\n\t"
    "s_add_u32 s29, s29, 4194304\n\t"
    "v_lshl_add_u32 v63, v0, 4, s29\n\t"
    "s_mov_b32 s22, 1179648\n\t"
    "s_mul_i32 s29, s36, 9216\n\t"
    "s_add_u32 s20, s29, s86\n\t"
    "s_addc_u32 s21, s87, 0\n\t"
    "buffer_load_dwordx4 v[234:237], v16, s[20:23], 0 offen\n\t"
    "buffer_load_dwordx4 v[56:59], v15, s[20:23], 0 offen\n\t"
    "s_waitcnt vmcnt(0)\n\t"
    "v_lshl_add_u64 v[234:235], v[234:235], 4, v[56:57]\n\t"
    "v_lshl_add_u64 v[236:237], v[236:237], 4, v[58:59]\n\t"
    "buffer_load_dwordx4 v[56:59], v14, s[20:23], 0 offen\n\t"
    "s_waitcnt vmcnt(0)\n\t"
    "v_lshl_add_u64 v[234:235], v[234:235], 4, v[56:57]\n\t"
    "v_lshl_add_u64 v[236:237], v[236:237], 4, v[58:59]\n\t"
    "buffer_load_dwordx4 v[56:59], v13, s[20:23], 0 offen\n\t"
    "s_waitcnt vmcnt(0)\n\t"
    "v_lshl_add_u64 v[234:235], v[234:235], 4, v[56:57]\n\t"
    "v_lshl_add_u64 v[236:237], v[236:237], 4, v[58:59]\n\t"
    "buffer_load_dwordx4 v[56:59], v12, s[20:23], 0 offen\n\t"
    "s_waitcnt vmcnt(0)\n\t"
    "v_lshl_add_u64 v[234:235], v[234:235], 4, v[56:57]\n\t"
    "v_lshl_add_u64 v[236:237], v[236:237], 4, v[58:59]\n\t"
    "buffer_load_dwordx4 v[56:59], v11, s[20:23], 0 offen\n\t"
    "s_waitcnt vmcnt(0)\n\t"
    "v_lshl_add_u64 v[234:235], v[234:235], 4, v[56:57]\n\t"
    "v_lshl_add_u64 v[236:237], v[236:237], 4, v[58:59]\n\t"
    "buffer_load_dwordx4 v[56:59], v10, s[20:23], 0 offen\n\t"
    "s_waitcnt vmcnt(0)\n\t"
    "v_lshl_add_u64 v[234:235], v[234:235], 4, v[56:57]\n\t"
    "v_lshl_add_u64 v[236:237], v[236:237], 4, v[58:59]\n\t"
    "buffer_load_dwordx4 v[56:59], v1, s[20:23], 0 offen\n\t"
    "s_waitcnt vmcnt(0)\n\t"
    "v_lshl_add_u64 v[234:235], v[234:235], 4, v[56:57]\n\t"
    "v_lshl_add_u64 v[236:237], v[236:237], 4, v[58:59]\n\t"
    "s_mov_b64 vcc, s[58:59]\n\t"
    "v_cndmask_b32_dpp v238, v234, v236, vcc quad_perm:[1,0,3,2] row_mask:0xf bank_mask:0xf\n\t"
    "v_cndmask_b32_dpp v239, v235, v237, vcc quad_perm:[1,0,3,2] row_mask:0xf bank_mask:0xf\n\t"
    "s_mov_b64 vcc, s[56:57]\n\t"
    "v_cndmask_b32_dpp v240, v236, v234, vcc quad_perm:[1,0,3,2] row_mask:0xf bank_mask:0xf\n\t"
    "v_cndmask_b32_dpp v241, v237, v235, vcc quad_perm:[1,0,3,2] row_mask:0xf bank_mask:0xf\n\t"
    "s_mov_b64 vcc, s[60:61]\n\t"
    "v_cndmask_b32_dpp v242, v240, v238, vcc row_shl:4 row_mask:0xf bank_mask:0xf bound_ctrl:1\n\t"
    "v_cndmask_b32_dpp v243, v241, v239, vcc row_shl:4 row_mask:0xf bank_mask:0xf bound_ctrl:1\n\t"
    "v_mov_b32_dpp v240, v238 row_shr:4 row_mask:0xf bank_mask:0xa\n\t"
    "v_mov_b32_dpp v241, v239 row_shr:4 row_mask:0xf bank_mask:0xa\n\t"
    "buffer_store_dwordx4 v[240:243], v63, s[92:95], s45 offen sc1\n\t"
    "s_mul_i32 s29, s37, 9216\n\t"
    "s_add_u32 s20, s29, s86\n\t"
    "s_addc_u32 s21, s87, 0\n\t"
    "buffer_load_dwordx4 v[234:237], v16, s[20:23], 0 offen\n\t"
    "buffer_load_dwordx4 v[56:59], v15, s[20:23], 0 offen\n\t"
    "s_waitcnt vmcnt(0)\n\t"
    "v_lshl_add_u64 v[234:235], v[234:235], 4, v[56:57]\n\t"
    "v_lshl_add_u64 v[236:237], v[236:237], 4, v[58:59]\n\t"
    "buffer_load_dwordx4 v[56:59], v14, s[20:23], 0 offen\n\t"
    "s_waitcnt vmcnt(0)\n\t"
    "v_lshl_add_u64 v[234:235], v[234:235], 4, v[56:57]\n\t"
    "v_lshl_add_u64 v[236:237], v[236:237], 4, v[58:59]\n\t"
    "buffer_load_dwordx4 v[56:59], v13, s[20:23], 0 offen\n\t"
    "s_waitcnt vmcnt(0)\n\t"
    "v_lshl_add_u64 v[234:235], v[234:235], 4, v[56:57]\n\t"
    "v_lshl_add_u64 v[236:237], v[236:237], 4, v[58:59]\n\t"
    "buffer_load_dwordx4 v[56:59], v12, s[20:23], 0 offen\n\t"
    "s_waitcnt vmcnt(0)\n\t"
    "v_lshl_add_u64 v[234:235], v[234:235], 4, v[56:57]\n\t"
    "v_lshl_add_u64 v[236:237], v[236:237], 4, v[58:59]\n\t"
    "buffer_load_dwordx4 v[56:59], v11, s[20:23], 0 offen\n\t"
    "s_waitcnt vmcnt(0)\n\t"
    "v_lshl_add_u64 v[234:235], v[234:235], 4, v[56:57]\n\t"
    "v_lshl_add_u64 v[236:237], v[236:237], 4, v[58:59]\n\t"
    "buffer_load_dwordx4 v[56:59], v10, s[20:23], 0 offen\n\t"
    "s_waitcnt vmcnt(0)\n\t"
    "v_lshl_add_u64 v[234:235], v[234:235], 4, v[56:57]\n\t"
    "v_lshl_add_u64 v[236:237], v[236:237], 4, v[58:59]\n\t"
    "buffer_load_dwordx4 v[56:59], v1, s[20:23], 0 offen\n\t"
    "s_waitcnt vmcnt(0)\n\t"
    "v_lshl_add_u64 v[234:235], v[234:235], 4, v[56:57]\n\t"
    "v_lshl_add_u64 v[236:237], v[236:237], 4, v[58:59]\n\t"
    "s_mov_b64 vcc, s[58:59]\n\t"
    "v_cndmask_b32_dpp v238, v234, v236, vcc quad_perm:[1,0,3,2] row_mask:0xf bank_mask:0xf\n\t"
    "v_cndmask_b32_dpp v239, v235, v237, vcc quad_perm:[1,0,3,2] row_mask:0xf bank_mask:0xf\n\t"
    "s_mov_b64 vcc, s[56:57]\n\t"
    "v_cndmask_b32_dpp v240, v236, v234, vcc quad_perm:[1,0,3,2] row_mask:0xf bank_mask:0xf\n\t"
    "v_cndmask_b32_dpp v241, v237, v235, vcc quad_perm:[1,0,3,2] row_mask:0xf bank_mask:0xf\n\t"
    "s_mov_b64 vcc, s[60:61]\n\t"
    "v_cndmask_b32_dpp v242, v240, v238, vcc row_shl:4 row_mask:0xf bank_mask:0xf bound_ctrl:1\n\t"
    "v_cndmask_b32_dpp v243, v241, v239, vcc row_shl:4 row_mask:0xf bank_mask:0xf bound_ctrl:1\n\t"
    "v_mov_b32_dpp v240, v238 row_shr:4 row_mask:0xf bank_mask:0xa\n\t"
    "v_mov_b32_dpp v241, v239 row_shr:4 row_mask:0xf bank_mask:0xa\n\t"
    "buffer_store_dwordx4 v[240:243], v63, s[92:95], s46 offen sc1\n\t"
    "s_mul_i32 s29, s38, 9216\n\t"
    "s_add_u32 s20, s29, s86\n\t"
    "s_addc_u32 s21, s87, 0\n\t"
    "buffer_load_dwordx4 v[234:237], v16, s[20:23], 0 offen\n\t"
    "buffer_load_dwordx4 v[56:59], v15, s[20:23], 0 offen\n\t"
    "s_waitcnt vmcnt(0)\n\t"
    "v_lshl_add_u64 v[234:235], v[234:235], 4, v[56:57]\n\t"
    "v_lshl_add_u64 v[236:237], v[236:237], 4, v[58:59]\n\t"
    "buffer_load_dwordx4 v[56:59], v14, s[20:23], 0 offen\n\t"
    "s_waitcnt vmcnt(0)\n\t"
    "v_lshl_add_u64 v[234:235], v[234:235], 4, v[56:57]\n\t"
    "v_lshl_add_u64 v[236:237], v[236:237], 4, v[58:59]\n\t"
    "buffer_load_dwordx4 v[56:59], v13, s[20:23], 0 offen\n\t"
    "s_waitcnt vmcnt(0)\n\t"
    "v_lshl_add_u64 v[234:235], v[234:235], 4, v[56:57]\n\t"
    "v_lshl_add_u64 v[236:237], v[236:237], 4, v[58:59]\n\t"
    "buffer_load_dwordx4 v[56:59], v12, s[20:23], 0 offen\n\t"
    "s_waitcnt vmcnt(0)\n\t"
    "v_lshl_add_u64 v[234:235], v[234:235], 4, v[56:57]\n\t"
    "v_lshl_add_u64 v[236:237], v[236:237], 4, v[58:59]\n\t"
    "buffer_load_dwordx4 v[56:59], v11, s[20:23], 0 offen\n\t"
    "s_waitcnt vmcnt(0)\n\t"
    "v_lshl_add_u64 v[234:235], v[234:235], 4, v[56:57]\n\t"
    "v_lshl_add_u64 v[236:237], v[236:237], 4, v[58:59]\n\t"
    "buffer_load_dwordx4 v[56:59], v10, s[20:23], 0 offen\n\t"
    "s_waitcnt vmcnt(0)\n\t"
    "v_lshl_add_u64 v[234:235], v[234:235], 4, v[56:57]\n\t"
    "v_lshl_add_u64 v[236:237], v[236:237], 4, v[58:59]\n\t"
    "buffer_load_dwordx4 v[56:59], v1, s[20:23], 0 offen\n\t"
    "s_waitcnt vmcnt(0)\n\t"
    "v_lshl_add_u64 v[234:235], v[234:235], 4, v[56:57]\n\t"
    "v_lshl_add_u64 v[236:237], v[236:237], 4, v[58:59]\n\t"
    "s_mov_b64 vcc, s[58:59]\n\t"
    "v_cndmask_b32_dpp v238, v234, v236, vcc quad_perm:[1,0,3,2] row_mask:0xf bank_mask:0xf\n\t"
    "v_cndmask_b32_dpp v239, v235, v237, vcc quad_perm:[1,0,3,2] row_mask:0xf bank_mask:0xf\n\t"
    "s_mov_b64 vcc, s[56:57]\n\t"
    "v_cndmask_b32_dpp v240, v236, v234, vcc quad_perm:[1,0,3,2] row_mask:0xf bank_mask:0xf\n\t"
    "v_cndmask_b32_dpp v241, v237, v235, vcc quad_perm:[1,0,3,2] row_mask:0xf bank_mask:0xf\n\t"
    "s_mov_b64 vcc, s[60:61]\n\t"
    "v_cndmask_b32_dpp v242, v240, v238, vcc row_shl:4 row_mask:0xf bank_mask:0xf bound_ctrl:1\n\t"
    "v_cndmask_b32_dpp v243, v241, v239, vcc row_shl:4 row_mask:0xf bank_mask:0xf bound_ctrl:1\n\t"
    "v_mov_b32_dpp v240, v238 row_shr:4 row_mask:0xf bank_mask:0xa\n\t"
    "v_mov_b32_dpp v241, v239 row_shr:4 row_mask:0xf bank_mask:0xa\n\t"
    "buffer_store_dwordx4 v[240:243], v63, s[92:95], s47 offen sc1\n\t"
    "s_mul_i32 s29, s39, 9216\n\t"
    "s_add_u32 s20, s29, s86\n\t"
    "s_addc_u32 s21, s87, 0\n\t"
    "buffer_load_dwordx4 v[234:237], v16, s[20:23], 0 offen\n\t"
    "buffer_load_dwordx4 v[56:59], v15, s[20:23], 0 offen\n\t"
    "s_waitcnt vmcnt(0)\n\t"
    "v_lshl_add_u64 v[234:235], v[234:235], 4, v[56:57]\n\t"
    "v_lshl_add_u64 v[236:237], v[236:237], 4, v[58:59]\n\t"
    "buffer_load_dwordx4 v[56:59], v14, s[20:23], 0 offen\n\t"
    "s_waitcnt vmcnt(0)\n\t"
    "v_lshl_add_u64 v[234:235], v[234:235], 4, v[56:57]\n\t"
    "v_lshl_add_u64 v[236:237], v[236:237], 4, v[58:59]\n\t"
    "buffer_load_dwordx4 v[56:59], v13, s[20:23], 0 offen\n\t"
    "s_waitcnt vmcnt(0)\n\t"
    "v_lshl_add_u64 v[234:235], v[234:235], 4, v[56:57]\n\t"
    "v_lshl_add_u64 v[236:237], v[236:237], 4, v[58:59]\n\t"
    "buffer_load_dwordx4 v[56:59], v12, s[20:23], 0 offen\n\t"
    "s_waitcnt vmcnt(0)\n\t"
    "v_lshl_add_u64 v[234:235], v[234:235], 4, v[56:57]\n\t"
    "v_lshl_add_u64 v[236:237], v[236:237], 4, v[58:59]\n\t"
    "buffer_load_dwordx4 v[56:59], v11, s[20:23], 0 offen\n\t"
    "s_waitcnt vmcnt(0)\n\t"
    "v_lshl_add_u64 v[234:235], v[234:235], 4, v[56:57]\n\t"
    "v_lshl_add_u64 v[236:237], v[236:237], 4, v[58:59]\n\t"
    "buffer_load_dwordx4 v[56:59], v10, s[20:23], 0 offen\n\t"
    "s_waitcnt vmcnt(0)\n\t"
    "v_lshl_add_u64 v[234:235], v[234:235], 4, v[56:57]\n\t"
    "v_lshl_add_u64 v[236:237], v[236:237], 4, v[58:59]\n\t"
    "buffer_load_dwordx4 v[56:59], v1, s[20:23], 0 offen\n\t"
    "s_waitcnt vmcnt(0)\n\t"
    "v_lshl_add_u64 v[234:235], v[234:235], 4, v[56:57]\n\t"
    "v_lshl_add_u64 v[236:237], v[236:237], 4, v[58:59]\n\t"
    "s_mov_b64 vcc, s[58:59]\n\t"
    "v_cndmask_b32_dpp v238, v234, v236, vcc quad_perm:[1,0,3,2] row_mask:0xf bank_mask:0xf\n\t"
    "v_cndmask_b32_dpp v239, v235, v237, vcc quad_perm:[1,0,3,2] row_mask:0xf bank_mask:0xf\n\t"
    "s_mov_b64 vcc, s[56:57]\n\t"
    "v_cndmask_b32_dpp v240, v236, v234, vcc quad_perm:[1,0,3,2] row_mask:0xf bank_mask:0xf\n\t"
    "v_cndmask_b32_dpp v241, v237, v235, vcc quad_perm:[1,0,3,2] row_mask:0xf bank_mask:0xf\n\t"
    "s_mov_b64 vcc, s[60:61]\n\t"
    "v_cndmask_b32_dpp v242, v240, v238, vcc row_shl:4 row_mask:0xf bank_mask:0xf bound_ctrl:1\n\t"
    "v_cndmask_b32_dpp v243, v241, v239, vcc row_shl:4 row_mask:0xf bank_mask:0xf bound_ctrl:1\n\t"
    "v_mov_b32_dpp v240, v238 row_shr:4 row_mask:0xf bank_mask:0xa\n\t"
    "v_mov_b32_dpp v241, v239 row_shr:4 row_mask:0xf bank_mask:0xa\n\t"
    "buffer_store_dwordx4 v[240:243], v63, s[92:95], s48 offen sc1\n\t"
    "s_waitcnt vmcnt(0)\n\t"
    "s_mov_b64 vcc, s[96:97]\n\t"
    "s_branch L_join0_%=\n\t"
    "L_fb1_%=:\n\t"
    "s_mov_b32 s33, 0\n\t"
    "s_mov_b64 s[96:97], vcc\n\t"
    "s_mul_i32 s29, s2, 36864\n\t"
    "s_add_u32 s29, s29, 4194304\n\t"
    "v_lshl_add_u32 v249, v0, 4, s29\n\t"
    "s_mov_b32 s22, 1179648\n\t"
    "s_mul_i32 s29, s40, 9216\n\t"
    "s_add_u32 s20, s29, s86\n\t"
    "s_addc_u32 s21, s87, 0\n\t"
    "buffer_load_dwordx4 v[234:237], v16, s[20:23], 0 offen\n\t"
    "buffer_load_dwordx4 v[56:59], v15, s[20:23], 0 offen\n\t"
    "s_waitcnt vmcnt(0)\n\t"
    "v_lshl_add_u64 v[234:235], v[234:235], 4, v[56:57]\n\t"
    "v_lshl_add_u64 v[236:237], v[236:237], 4, v[58:59]\n\t"
    "buffer_load_dwordx4 v[56:59], v14, s[20:23], 0 offen\n\t"
    "s_waitcnt vmcnt(0)\n\t"
    "v_lshl_add_u64 v[234:235], v[234:235], 4, v[56:57]\n\t"
    "v_lshl_add_u64 v[236:237], v[236:237], 4, v[58:59]\n\t"
    "buffer_load_dwordx4 v[56:59], v13, s[20:23], 0 offen\n\t"
    "s_waitcnt vmcnt(0)\n\t"
    "v_lshl_add_u64 v[234:235], v[234:235], 4, v[56:57]\n\t"
    "v_lshl_add_u64 v[236:237], v[236:237], 4, v[58:59]\n\t"
    "buffer_load_dwordx4 v[56:59], v12, s[20:23], 0 offen\n\t"
    "s_waitcnt vmcnt(0)\n\t"
    "v_lshl_add_u64 v[234:235], v[234:235], 4, v[56:57]\n\t"
    "v_lshl_add_u64 v[236:237], v[236:237], 4, v[58:59]\n\t"
    "buffer_load_dwordx4 v[56:59], v11, s[20:23], 0 offen\n\t"
    "s_waitcnt vmcnt(0)\n\t"
    "v_lshl_add_u64 v[234:235], v[234:235], 4, v[56:57]\n\t"
    "v_lshl_add_u64 v[236:237], v[236:237], 4, v[58:59]\n\t"
    "buffer_load_dwordx4 v[56:59], v10, s[20:23], 0 offen\n\t"
    "s_waitcnt vmcnt(0)\n\t"
    "v_lshl_add_u64 v[234:235], v[234:235], 4, v[56:57]\n\t"
    "v_lshl_add_u64 v[236:237], v[236:237], 4, v[58:59]\n\t"
    "buffer_load_dwordx4 v[56:59], v1, s[20:23], 0 offen\n\t"
    "s_waitcnt vmcnt(0)\n\t"
    "v_lshl_add_u64 v[234:235], v[234:235], 4, v[56:57]\n\t"
    "v_lshl_add_u64 v[236:237], v[236:237], 4, v[58:59]\n\t"
    "s_mov_b64 vcc, s[58:59]\n\t"
    "v_cndmask_b32_dpp v238, v234, v236, vcc quad_perm:[1,0,3,2] row_mask:0xf bank_mask:0xf\n\t"
    "v_cndmask_b32_dpp v239, v235, v237, vcc quad_perm:[1,0,3,2] row_mask:0xf bank_mask:0xf\n\t"
    "s_mov_b64 vcc, s[56:57]\n\t"
    "v_cndmask_b32_dpp v240, v236, v234, vcc quad_perm:[1,0,3,2] row_mask:0xf bank_mask:0xf\n\t"
    "v_cndmask_b32_dpp v241, v237, v235, vcc quad_perm:[1,0,3,2] row_mask:0xf bank_mask:0xf\n\t"
    "s_mov_b64 vcc, s[60:61]\n\t"
    "v_cndmask_b32_dpp v242, v240, v238, vcc row_shl:4 row_mask:0xf bank_mask:0xf bound_ctrl:1\n\t"
    "v_cndmask_b32_dpp v243, v241, v239, vcc row_shl:4 row_mask:0xf bank_mask:0xf bound_ctrl:1\n\t"
    "v_mov_b32_dpp v240, v238 row_shr:4 row_mask:0xf bank_mask:0xa\n\t"
    "v_mov_b32_dpp v241, v239 row_shr:4 row_mask:0xf bank_mask:0xa\n\t"
    "buffer_store_dwordx4 v[240:243], v249, s[92:95], s49 offen sc1\n\t"
    "s_mul_i32 s29, s41, 9216\n\t"
    "s_add_u32 s20, s29, s86\n\t"
    "s_addc_u32 s21, s87, 0\n\t"
    "buffer_load_dwordx4 v[234:237], v16, s[20:23], 0 offen\n\t"
    "buffer_load_dwordx4 v[56:59], v15, s[20:23], 0 offen\n\t"
    "s_waitcnt vmcnt(0)\n\t"
    "v_lshl_add_u64 v[234:235], v[234:235], 4, v[56:57]\n\t"
    "v_lshl_add_u64 v[236:237], v[236:237], 4, v[58:59]\n\t"
    "buffer_load_dwordx4 v[56:59], v14, s[20:23], 0 offen\n\t"
    "s_waitcnt vmcnt(0)\n\t"
    "v_lshl_add_u64 v[234:235], v[234:235], 4, v[56:57]\n\t"
    "v_lshl_add_u64 v[236:237], v[236:237], 4, v[58:59]\n\t"
    "buffer_load_dwordx4 v[56:59], v13, s[20:23], 0 offen\n\t"
    "s_waitcnt vmcnt(0)\n\t"
    "v_lshl_add_u64 v[234:235], v[234:235], 4, v[56:57]\n\t"
    "v_lshl_add_u64 v[236:237], v[236:237], 4, v[58:59]\n\t"
    "buffer_load_dwordx4 v[56:59], v12, s[20:23], 0 offen\n\t"
    "s_waitcnt vmcnt(0)\n\t"
    "v_lshl_add_u64 v[234:235], v[234:235], 4, v[56:57]\n\t"
    "v_lshl_add_u64 v[236:237], v[236:237], 4, v[58:59]\n\t"
    "buffer_load_dwordx4 v[56:59], v11, s[20:23], 0 offen\n\t"
    "s_waitcnt vmcnt(0)\n\t"
    "v_lshl_add_u64 v[234:235], v[234:235], 4, v[56:57]\n\t"
    "v_lshl_add_u64 v[236:237], v[236:237], 4, v[58:59]\n\t"
    "buffer_load_dwordx4 v[56:59], v10, s[20:23], 0 offen\n\t"
    "s_waitcnt vmcnt(0)\n\t"
    "v_lshl_add_u64 v[234:235], v[234:235], 4, v[56:57]\n\t"
    "v_lshl_add_u64 v[236:237], v[236:237], 4, v[58:59]\n\t"
    "buffer_load_dwordx4 v[56:59], v1, s[20:23], 0 offen\n\t"
    "s_waitcnt vmcnt(0)\n\t"
    "v_lshl_add_u64 v[234:235], v[234:235], 4, v[56:57]\n\t"
    "v_lshl_add_u64 v[236:237], v[236:237], 4, v[58:59]\n\t"
    "s_mov_b64 vcc, s[58:59]\n\t"
    "v_cndmask_b32_dpp v238, v234, v236, vcc quad_perm:[1,0,3,2] row_mask:0xf bank_mask:0xf\n\t"
    "v_cndmask_b32_dpp v239, v235, v237, vcc quad_perm:[1,0,3,2] row_mask:0xf bank_mask:0xf\n\t"
    "s_mov_b64 vcc, s[56:57]\n\t"
    "v_cndmask_b32_dpp v240, v236, v234, vcc quad_perm:[1,0,3,2] row_mask:0xf bank_mask:0xf\n\t"
    "v_cndmask_b32_dpp v241, v237, v235, vcc quad_perm:[1,0,3,2] row_mask:0xf bank_mask:0xf\n\t"
    "s_mov_b64 vcc, s[60:61]\n\t"
    "v_cndmask_b32_dpp v242, v240, v238, vcc row_shl:4 row_mask:0xf bank_mask:0xf bound_ctrl:1\n\t"
    "v_cndmask_b32_dpp v243, v241, v239, vcc row_shl:4 row_mask:0xf bank_mask:0xf bound_ctrl:1\n\t"
    "v_mov_b32_dpp v240, v238 row_shr:4 row_mask:0xf bank_mask:0xa\n\t"
    "v_mov_b32_dpp v241, v239 row_shr:4 row_mask:0xf bank_mask:0xa\n\t"
    "buffer_store_dwordx4 v[240:243], v249, s[92:95], s50 offen sc1\n\t"
    "s_mul_i32 s29, s42, 9216\n\t"
    "s_add_u32 s20, s29, s86\n\t"
    "s_addc_u32 s21, s87, 0\n\t"
    "buffer_load_dwordx4 v[234:237], v16, s[20:23], 0 offen\n\t"
    "buffer_load_dwordx4 v[56:59], v15, s[20:23], 0 offen\n\t"
    "s_waitcnt vmcnt(0)\n\t"
    "v_lshl_add_u64 v[234:235], v[234:235], 4, v[56:57]\n\t"
    "v_lshl_add_u64 v[236:237], v[236:237], 4, v[58:59]\n\t"
    "buffer_load_dwordx4 v[56:59], v14, s[20:23], 0 offen\n\t"
    "s_waitcnt vmcnt(0)\n\t"
    "v_lshl_add_u64 v[234:235], v[234:235], 4, v[56:57]\n\t"
    "v_lshl_add_u64 v[236:237], v[236:237], 4, v[58:59]\n\t"
    "buffer_load_dwordx4 v[56:59], v13, s[20:23], 0 offen\n\t"
    "s_waitcnt vmcnt(0)\n\t"
    "v_lshl_add_u64 v[234:235], v[234:235], 4, v[56:57]\n\t"
    "v_lshl_add_u64 v[236:237], v[236:237], 4, v[58:59]\n\t"
    "buffer_load_dwordx4 v[56:59], v12, s[20:23], 0 offen\n\t"
    "s_waitcnt vmcnt(0)\n\t"
    "v_lshl_add_u64 v[234:235], v[234:235], 4, v[56:57]\n\t"
    "v_lshl_add_u64 v[236:237], v[236:237], 4, v[58:59]\n\t"
    "buffer_load_dwordx4 v[56:59], v11, s[20:23], 0 offen\n\t"
    "s_waitcnt vmcnt(0)\n\t"
    "v_lshl_add_u64 v[234:235], v[234:235], 4, v[56:57]\n\t"
    "v_lshl_add_u64 v[236:237], v[236:237], 4, v[58:59]\n\t"
    "buffer_load_dwordx4 v[56:59], v10, s[20:23], 0 offen\n\t"
    "s_waitcnt vmcnt(0)\n\t"
    "v_lshl_add_u64 v[234:235], v[234:235], 4, v[56:57]\n\t"
    "v_lshl_add_u64 v[236:237], v[236:237], 4, v[58:59]\n\t"
    "buffer_load_dwordx4 v[56:59], v1, s[20:23], 0 offen\n\t"
    "s_waitcnt vmcnt(0)\n\t"
    "v_lshl_add_u64 v[234:235], v[234:235], 4, v[56:57]\n\t"
    "v_lshl_add_u64 v[236:237], v[236:237], 4, v[58:59]\n\t"
    "s_mov_b64 vcc, s[58:59]\n\t"
    "v_cndmask_b32_dpp v238, v234, v236, vcc quad_perm:[1,0,3,2] row_mask:0xf bank_mask:0xf\n\t"
    "v_cndmask_b32_dpp v239, v235, v237, vcc quad_perm:[1,0,3,2] row_mask:0xf bank_mask:0xf\n\t"
    "s_mov_b64 vcc, s[56:57]\n\t"
    "v_cndmask_b32_dpp v240, v236, v234, vcc quad_perm:[1,0,3,2] row_mask:0xf bank_mask:0xf\n\t"
    "v_cndmask_b32_dpp v241, v237, v235, vcc quad_perm:[1,0,3,2] row_mask:0xf bank_mask:0xf\n\t"
    "s_mov_b64 vcc, s[60:61]\n\t"
    "v_cndmask_b32_dpp v242, v240, v238, vcc row_shl:4 row_mask:0xf bank_mask:0xf bound_ctrl:1\n\t"
    "v_cndmask_b32_dpp v243, v241, v239, vcc row_shl:4 row_mask:0xf bank_mask:0xf bound_ctrl:1\n\t"
    "v_mov_b32_dpp v240, v238 row_shr:4 row_mask:0xf bank_mask:0xa\n\t"
    "v_mov_b32_dpp v241, v239 row_shr:4 row_mask:0xf bank_mask:0xa\n\t"
    "buffer_store_dwordx4 v[240:243], v249, s[92:95], s51 offen sc1\n\t"
    "s_waitcnt vmcnt(0)\n\t"
    "s_mov_b64 vcc, s[96:97]\n\t"
    "s_branch L_join1_%=\n\t"
    "L_exit_%=:\n\t"
    :
    : "{s[0:1]}"(__builtin_amdgcn_kernarg_segment_ptr()), "{s2}"(blockIdx.x), "{v0}"(threadIdx.x), "s"((unsigned)(size_t)lds)
    : "v1", "v2", "v3", "v4", "v5", "v6", "v7", "v8", "v9", "v10", "v11", "v12", "v13", "v14", "v15", "v16", "v17", "v18", "v19", "v20", "v21", "v22", "v23", "v24", "v25", "v26", "v27", "v28", "v29", "v30", "v31", "v32", "v33", "v34", "v35", "v36", "v37", "v38", "v39", "v40", "v41", "v42", "v43", "v44", "v45", "v46", "v47", "v48", "v49", "v50", "v51", "v52", "v53", "v54", "v55", "v56", "v57", "v58", "v59", "v60", "v61", "v62", "v63", "v64", "v65", "v66", "v67", "v68", "v69", "v70", "v71", "v72", "v73", "v74", "v75", "v76", "v77", "v78", "v79", "v80", "v81", "v82", "v83", "v84", "v85", "v86", "v87", "v88", "v89", "v90", "v91", "v92", "v93", "v94", "v95", "v96", "v97", "v98", "v99", "v100", "v101", "v102", "v103", "v104", "v105", "v106", "v107", "v108", "v109", "v110", "v111", "v112", "v113", "v114", "v115", "v116", "v117", "v118", "v119", "v120", "v121", "v122", "v123", "v124", "v125", "v126", "v127", "v128", "v129", "v130", "v131", "v132", "v133", "v134", "v135", "v136", "v137", "v138", "v139", "v140", "v141", "v142", "v143", "v144", "v145", "v146", "v147", "v148", "v149", "v150", "v151", "v152", "v153", "v154", "v155", "v156", "v157", "v158", "v159", "v160", "v161", "v162", "v163", "v164", "v165", "v166", "v167", "v168", "v169", "v170", "v171", "v172", "v173", "v174", "v175", "v176", "v177", "v178", "v179", "v180", "v181", "v182", "v183", "v184", "v185", "v186", "v187", "v188", "v189", "v190", "v191", "v192", "v193", "v194", "v195", "v196", "v197", "v198", "v199", "v200", "v201", "v202", "v203", "v204", "v205", "v206", "v207", "v208", "v209", "v210", "v211", "v212", "v213", "v214", "v215", "v216", "v217", "v218", "v219", "v220", "v221", "v222", "v223", "v224", "v225", "v226", "v227", "v228", "v229", "v230", "v231", "v232", "v233", "v234", "v235", "v236", "v237", "v238", "v239", "v240", "v241", "v242", "v243", "v244", "v245", "v246", "v247", "v248", "v249", "v250", "v251", "v252", "v253", "a0", "a1", "a2", "a3", "a4", "a5", "a6", "a7", "a8", "a9", "a10", "a11", "a12", "a13", "a14", "a15", "a16", "a17", "a18", "a19", "a20", "a21", "a22", "a23", "a24", "a25", "a26", "a27", "a28", "a29", "a30", "a31", "a32", "a33", "a34", "a35", "a36", "a37", "a38", "a39", "a40", "a41", "a42", "a43", "a44", "a45", "a46", "a47", "a48", "a49", "a50", "a51", "a52", "a53", "a54", "a55", "a56", "a57", "a58", "a59", "a60", "a61", "a62", "a63", "a64", "a65", "a66", "a67", "a68", "a69", "a70", "a71", "a72", "a73", "a74", "a75", "a76", "a77", "a78", "a79", "a80", "a81", "a82", "a83", "a84", "a85", "a86", "a87", "a88", "a89", "a90", "a91", "a92", "a93", "a94", "a95", "a96", "a97", "a98", "a99", "a100", "a101", "a102", "a103", "a104", "a105", "a106", "a107", "a108", "a109", "a110", "a111", "a112", "a113", "a114", "a115", "a116", "a117", "a118", "a119", "a120", "a121", "a122", "a123", "a124", "a125", "a126", "a127", "a128", "a129", "a130", "a131", "a132", "a133", "a134", "a135", "a136", "a137", "a138", "a139", "a140", "a141", "a142", "a143", "a144", "a145", "a146", "a147", "a148", "a149", "a150", "a151", "a152", "a153", "a154", "a155", "a156", "a157", "a158", "a159", "a160", "a161", "a162", "a163", "a164", "a165", "a166", "a167", "a168", "a169", "a170", "a171", "a172", "a173", "a174", "a175", "a176", "a177", "a178", "a179", "a180", "a181", "a182", "a183", "a184", "a185", "a186", "a187", "a188", "a189", "a190", "a191", "a192", "a193", "a194", "a195", "a196", "a197", "a198", "a199", "a200", "a201", "a202", "a203", "a204", "a205", "a206", "a207", "a208", "a209", "a210", "a211", "a212", "a213", "a214", "a215", "a216", "a217", "a218", "a219", "a220", "a221", "a222", "a223", "a224", "a225", "a226", "a227", "a228", "a229", "a230", "a231", "a232", "a233", "a234", "a235", "a236", "a237", "a238", "a239", "a240", "a241", "a242", "a243", "a244", "a245", "a246", "a247", "a248", "a249", "a250", "a251", "a252", "a253", "a254", "a255", "s4", "s5", "s6", "s7", "s8", "s9", "s10", "s11", "s12", "s13", "s14", "s15", "s16", "s17", "s18", "s19", "s20", "s21", "s22", "s23", "s24", "s25", "s26", "s27", "s28", "s29", "s30", "s31", "s33", "s34", "s35", "s36", "s37", "s38", "s39", "s40", "s41", "s42", "s43", "s44", "s45", "s46", "s47", "s48", "s49", "s50", "s51", "s52", "s53", "s54", "s55", "s56", "s57", "s58", "s59", "s60", "s61", "s62", "s63", "s64", "s65", "s66", "s67", "s68", "s69", "s70", "s71", "s72", "s73", "s74", "s75", "s76", "s77", "s78", "s79", "s80", "s81", "s82", "s83", "s84", "s85", "s86", "s87", "s88", "s89", "s90", "s91", "s92", "s93", "s94", "s95", "s96", "s97", "s98", "s99", "vcc", "scc", "memory");
}

extern "C" void kernel_launch(void* const* d_in, const int* in_sizes, int n_in, void* d_out, int out_size, void* d_ws, size_t ws_size, hipStream_t stream) {
  if (n_in != 3 || in_sizes[0] != 1179648 || in_sizes[1] != 2654208 || in_sizes[2] != 4608 || out_size != 9437184 || ws_size < 13631488) return;
  nf4_gemm<<<256, 256, 0, stream>>>(d_in[0], d_in[1], d_in[2], d_out, d_ws);
}
```
